```python
import torch

def _softmax(x, axis=-1):
    return torch.softmax(x, dim=axis)
def _gelu(x):
    return torch.nn.functional.gelu(x, approximate="tanh")
def _silu(x):
    return torch.nn.functional.silu(x)
def _sigmoid(x):
    return torch.sigmoid(x)
def _layernorm(x, axis=-1, eps=1e-5):
    m = x.mean(dim=axis, keepdim=True)
    v = x.var(dim=axis, keepdim=True, unbiased=False)
    return (x - m) / torch.sqrt(v + eps)
def _scatter_add(tgt, idx, src, axis=0):
    return tgt.clone().index_add_(axis, idx, src)
def _segment_sum(src, seg, n):
    out = torch.zeros((n,) + src.shape[1:], dtype=src.dtype)
    return out.index_add_(0, seg, src)
def _pad(x, pw):
    flat = tuple(v for pair in reversed(pw) for v in pair)
    return torch.nn.functional.pad(x, flat)

kernel_name = 'parallel_sum__linear_attn_block__mixer_channel_mlp__prompt'

def setup_inputs():

    _w = (lambda a: (lambda t: t.to(torch.bfloat16) if t.is_floating_point() else t)(torch.as_tensor(a)))
    def _A_setup():
        B, S, D = 1, 2048, 448
        return {
            "x": _w((lambda *s: torch.randn(*s, dtype=torch.bfloat16) * 0.05)(B, S, D)),
            "wqkv": _w((lambda *s: torch.randn(*s, dtype=torch.bfloat16) * 0.1)(D, 3 * D)),
            "wo": _w((lambda *s: torch.randn(*s, dtype=torch.bfloat16) * 0.05)(D, D)),
        }
    _A_in = _A_setup()

    def _B_setup():
        B, S, D, F = 1, 2048, 448, 1792
        return {
            "x": _w((lambda *s: torch.randn(*s, dtype=torch.bfloat16) * 0.05)(B, S, D)),
            "w1": _w((lambda *s: torch.randn(*s, dtype=torch.bfloat16) * 1.6)(D, F) * (D ** -0.5)),
            "b1": _w((lambda *s: torch.randn(*s, dtype=torch.bfloat16) * 0.4)(F)),
            "w2": _w((lambda *s: torch.randn(*s, dtype=torch.bfloat16) * 0.05)(F, D) * (F ** -0.5)),
            "b2": _w((lambda *s: torch.randn(*s, dtype=torch.bfloat16) * 0.05)(D)),
        }
    _B_in = _B_setup()

    return {
        "A_x": _A_in["x"],
        "A_wqkv": _A_in["wqkv"],
        "A_wo": _A_in["wo"],
        "B_x": _B_in["x"],
        "B_w1": _B_in["w1"],
        "B_b1": _B_in["b1"],
        "B_w2": _B_in["w2"],
        "B_b2": _B_in["b2"],
    }

def reference(A_x, A_wqkv, A_wo, B_x, B_w1, B_b1, B_w2, B_b2):
    def _A_ref(x, wqkv, wo):
        B, S, D = x.shape
        H = 16; Dh = D // H
        qkv = (lambda x, *d: x.permute(*d))((x @ wqkv).reshape(B, S, 3, H, Dh), 2, 0, 3, 1, 4)
        q, k, v = qkv[0], qkv[1], qkv[2]
        phi = lambda t: torch.where(t > 0, t + 1, torch.exp(t)) + 1e-6
        pq, pk = phi(q), phi(k)
        kv = torch.einsum("bhsd,bhse->bhde", pk, v)
        z = torch.einsum("bhsd->bhd", pk)
        num = torch.einsum("bhqd,bhde->bhqe", pq, kv)
        den = torch.einsum("bhqd,bhd->bhq", pq, z)[..., None] + 1e-6
        o = num / den
        return (lambda x, *d: x.permute(*d))(o, 0, 2, 1, 3).reshape(B, S, D) @ wo

    def _B_ref(x, w1, b1, w2, b2):
        y = _layernorm(x, axis=-1)
        h = _gelu(torch.einsum("bsd,df->bsf", y, w1) + b1)
        return torch.einsum("bsf,fd->bsd", h, w2) + b2

    x = A_x
    xn = _layernorm(x, axis=-1)
    return x + _A_ref(xn, A_wqkv, A_wo) + _B_ref(xn, B_w1, B_b1, B_w2, B_b2)
```

```cpp
#include <hip/hip_runtime.h>
#include <hip/hip_bf16.h>
typedef unsigned short u16;
__device__ __forceinline__ float b2f(u16 b){ return __uint_as_float(((unsigned)b)<<16); }
__device__ __forceinline__ u16 f2b(float f){ unsigned u=__float_as_uint(f); u += 0x7fffu + ((u>>16)&1u); return (u16)(u>>16); }
constexpr int S=2048, D=448, F=1792, H=16, DH=28;

__global__ void k_ln(const u16* x, float* xn, float* y){
  int t=blockIdx.x; int l=threadIdx.x;
  float v[7]; float s=0;
  for(int i=0;i<7;i++){ v[i]=b2f(x[t*D+l+64*i]); s+=v[i]; }
  for(int o=32;o;o>>=1) s+=__shfl_xor(s,o);
  float m=s/D; float q=0;
  for(int i=0;i<7;i++){ float d=v[i]-m; q+=d*d; }
  for(int o=32;o;o>>=1) q+=__shfl_xor(q,o);
  float r=1.f/sqrtf(q/D+1e-5f);
  s=0;
  for(int i=0;i<7;i++){ v[i]=(v[i]-m)*r; xn[t*D+l+64*i]=v[i]; s+=v[i]; }
  for(int o=32;o;o>>=1) s+=__shfl_xor(s,o);
  m=s/D; q=0;
  for(int i=0;i<7;i++){ float d=v[i]-m; q+=d*d; }
  for(int o=32;o;o>>=1) q+=__shfl_xor(q,o);
  r=1.f/sqrtf(q/D+1e-5f);
  for(int i=0;i<7;i++){ y[t*D+l+64*i]=(v[i]-m)*r; }
}
__global__ void k_gemm(const float* A, const u16* B, float* C, int M, int N, int K){
  int n=blockIdx.x*64+threadIdx.x; int m0=blockIdx.y*8;
  if(n>=N) return;
  float acc[8]={0,0,0,0,0,0,0,0};
  for(int k=0;k<K;k++){ float b=b2f(B[(size_t)k*N+n]);
    for(int i=0;i<8;i++) acc[i]+=A[(size_t)(m0+i)*K+k]*b; }
  for(int i=0;i<8;i++) C[(size_t)(m0+i)*N+n]=acc[i];
}
__device__ __forceinline__ float phi(float t){ return (t>0? t+1.f: expf(t))+1e-6f; }
__global__ void k_kv(const float* qkv, float* kv){
  int h=blockIdx.x; int d=threadIdx.x>>5, e=threadIdx.x&31;
  if(d>=DH||e>DH) return;
  float s=0;
  for(int t=0;t<S;t++){ float pk=phi(qkv[(size_t)t*3*D+D+h*DH+d]); float v= e<DH? qkv[(size_t)t*3*D+2*D+h*DH+e]:1.f; s+=pk*v; }
  kv[(h*32+d)*32+e]=s;
}
__global__ void k_o(const float* qkv, const float* kv, float* o){
  int t=blockIdx.x; int c=threadIdx.x; if(c>=D) return; int h=c/DH, e=c%DH;
  float num=0, den=0;
  for(int d=0;d<DH;d++){ float pq=phi(qkv[(size_t)t*3*D+h*DH+d]); num+=pq*kv[(h*32+d)*32+e]; den+=pq*kv[(h*32+d)*32+DH]; }
  o[(size_t)t*D+c]=num/(den+1e-6f);
}
__global__ void k_gelu(float* hbuf, const u16* b1){
  int i=blockIdx.x*256+threadIdx.x; float v=hbuf[i]+b2f(b1[i%F]);
  hbuf[i]=0.5f*v*(1.f+tanhf(0.7978845608028654f*(v+0.044715f*v*v*v)));
}
__global__ void k_out(const u16* x, const float* a, const float* b, const u16* b2, u16* out){
  int i=blockIdx.x*256+threadIdx.x; out[i]=f2b(b2f(x[i])+a[i]+b[i]+b2f(b2[i%D]));
}
extern "C" void kernel_launch(void* const* d_in, const int* in_sizes, int n_in, void* d_out, int out_size, void* d_ws, size_t ws_size, hipStream_t stream){
  const u16* x=(const u16*)d_in[0]; const u16* wqkv=(const u16*)d_in[1]; const u16* wo=(const u16*)d_in[2];
  const u16* w1=(const u16*)d_in[4]; const u16* b1=(const u16*)d_in[5]; const u16* w2=(const u16*)d_in[6]; const u16* b2=(const u16*)d_in[7];
  float* ws=(float*)d_ws;
  float* xn=ws; float* y=xn+S*D; float* qkv=y+S*D; float* kv=qkv+S*3*D; float* o=kv+16*1024; float* a=o+S*D; float* hb=a+S*D; float* bo=hb+S*F;
  k_ln<<<S,64,0,stream>>>(x,xn,y);
  k_gemm<<<dim3(3*D/64,S/8),64,0,stream>>>(xn,wqkv,qkv,S,3*D,D);
  k_kv<<<16,1024,0,stream>>>(qkv,kv);
  k_o<<<S,448,0,stream>>>(qkv,kv,o);
  k_gemm<<<dim3(D/64,S/8),64,0,stream>>>(o,wo,a,S,D,D);
  k_gemm<<<dim3(F/64,S/8),64,0,stream>>>(y,w1,hb,S,F,D);
  k_gelu<<<S*F/256,256,0,stream>>>(hb,b1);
  k_gemm<<<dim3(D/64,S/8),64,0,stream>>>(hb,w2,bo,S,D,F);
  k_out<<<S*D/256,256,0,stream>>>(x,a,bo,b2,(u16*)d_out);
}
```

```cpp
#include <hip/hip_runtime.h>
#pragma clang diagnostic ignored "-Wint-to-pointer-cast"

typedef unsigned long long u64;
typedef __attribute__((ext_vector_type(8))) __bf16 bf16x8;
typedef __attribute__((ext_vector_type(2))) __bf16 bf16x2;
typedef __attribute__((ext_vector_type(4))) short s16x4;
typedef __attribute__((ext_vector_type(16))) float f32x16;
typedef __attribute__((ext_vector_type(4))) float f32x4;
typedef __attribute__((ext_vector_type(2))) float f32x2;
typedef __attribute__((ext_vector_type(4))) unsigned u32x4;
typedef __attribute__((ext_vector_type(2))) unsigned u32x2;
typedef __amdgpu_buffer_rsrc_t rsrc_t;
#define GAS __attribute__((address_space(1)))
#define LAS __attribute__((address_space(3)))
#define DI __device__ __forceinline__
#define SCHED() __builtin_amdgcn_sched_barrier(0)
#define BAR() __builtin_amdgcn_s_barrier()
#define WAITV(n) asm volatile("s_waitcnt vmcnt(" #n ")" ::: "memory")
#define WAITL0() asm volatile("s_waitcnt lgkmcnt(0)" ::: "memory")

constexpr unsigned STG = 51456;
constexpr unsigned XOFF = 34816;
constexpr unsigned L_CSP = 154368;
constexpr unsigned L_ST = 158464;
constexpr unsigned L_CSF = 159488;
constexpr unsigned L_RS = 161536;
constexpr unsigned LDS_BYTES = 163840;
constexpr unsigned W_GEN = 0;
constexpr unsigned W_F1 = 4096;
constexpr unsigned W_FK = W_F1 + 256 * 128;
constexpr unsigned W_KVF = 65536;
constexpr unsigned W_KVP = 131072;
constexpr unsigned W_PQ = W_KVP + (1u << 20);
constexpr unsigned W_H = W_PQ + (2u << 20);
constexpr unsigned W_END = W_H + 64u * 112u * 1024u;
constexpr unsigned W_DBG = 16u << 20;
constexpr int SPIN_MAX = 400000;

#define STAMP(i) do {} while (0)
#define STAMP_OUT() do {} while (0)
#define CYC(i) do {} while (0)

DI void glds(unsigned ldsaddr, unsigned voff, rsrc_t r, unsigned soff) {
  asm volatile("s_mov_b32 m0, %0\n\ts_nop 0\n\tbuffer_load_dwordx4 %1, %2, %3 offen lds" ::"s"(ldsaddr), "v"(voff), "s"(r), "s"(soff) : "memory");
}
DI bf16x8 ld128(unsigned a) { return *(LAS bf16x8*)a; }
DI f32x4 ldf4(unsigned a) { return *(LAS f32x4*)a; }
DI bf16x8 ldtr(unsigned a, unsigned hi) {
  s16x4 lo = __builtin_amdgcn_ds_read_tr16_b64_v4i16((LAS s16x4*)a);
  s16x4 h = __builtin_amdgcn_ds_read_tr16_b64_v4i16((LAS s16x4*)(a + hi));
  return __builtin_bit_cast(bf16x8, __builtin_shufflevector(lo, h, 0, 1, 2, 3, 4, 5, 6, 7));
}
DI f32x16 mfma(bf16x8 a, bf16x8 b, f32x16 c) { return __builtin_amdgcn_mfma_f32_32x32x16_bf16(a, b, c, 0, 0, 0); }
DI float dot2(unsigned a, unsigned b, float c) { return __builtin_amdgcn_fdot2_f32_bf16(__builtin_bit_cast(bf16x2, a), __builtin_bit_cast(bf16x2, b), c, false); }
DI unsigned pk2(float a, float b) { f32x2 v = {a, b}; return __builtin_bit_cast(unsigned, __builtin_convertvector(v, bf16x2)); }
DI float halfsum(float x) { auto t = __builtin_amdgcn_permlane32_swap(__float_as_uint(x), __float_as_uint(x), false, false); return __uint_as_float(t[0]) + __uint_as_float(t[1]); }
DI float bcast_hi(float x) { auto t = __builtin_amdgcn_permlane32_swap(__float_as_uint(x), __float_as_uint(x), false, false); return __uint_as_float(t[1]); }
DI u32x4 ldsc(rsrc_t r, unsigned voff, unsigned soff) { return __builtin_amdgcn_raw_buffer_load_b128(r, voff, soff, 16); }
DI void stsc(u32x4 v, rsrc_t r, unsigned voff, unsigned soff) { __builtin_amdgcn_raw_buffer_store_b128(v, r, voff, soff, 16); }
DI float phi(float t) { return (t > 0.f ? t + 1.f : __builtin_amdgcn_exp2f(t * 1.4426950408889634f)) + 1e-6f; }
DI void wait_flag(const char* ws, unsigned off, u64 tag) {
  for (int s = 0; s < SPIN_MAX; ++s) {
    u64 v = __hip_atomic_load((GAS u64*)(ws + off), __ATOMIC_RELAXED, __HIP_MEMORY_SCOPE_AGENT);
    if (__all(v == tag)) break;
    __builtin_amdgcn_s_sleep(1);
  }
  __builtin_amdgcn_fence(__ATOMIC_ACQUIRE, "wavefront");
}

struct Ld {
  rsrc_t rx, rw, r2, ro;
  unsigned vx, vw, v2, v2b;
  unsigned rs;
  unsigned c0;
  unsigned w;
};
DI void issue_chunk(const Ld& L, unsigned i, unsigned sb) {
  unsigned c = L.c0 + i; c = c >= 7 ? c - 7 : c;
  unsigned sx = c * 128, sw = c * 64 * L.rs;
  unsigned lx = sb + XOFF + L.w * 2080, lw = sb + L.w * 4352;
  glds(lx, L.vx, L.rx, sx);
  glds(lw, L.vw, L.rw, sw);
  glds(lw + 1088, L.vw, L.rw, sw + 2 * L.rs);
  glds(lx + 1040, L.vx, L.rx, sx + 7168);
  glds(lw + 2176, L.vw, L.rw, sw + 4 * L.rs);
  glds(lw + 3264, L.vw, L.rw, sw + 6 * L.rs);
}
DI void issue_w2_h(const Ld& L, unsigned gi, unsigned reg) {
  unsigned l = reg * STG + L.w * 6144, s = (L.w * 224 + gi * 48) * 896;
#pragma unroll
  for (int j = 0; j < 6; ++j) glds(l + j * 1024, L.v2, L.r2, s + j * 7168);
}
DI void issue_w2_g4(const Ld& L) {
  unsigned l = STG + L.w * 6144, s = (L.w * 224 + 192) * 896, so = L.w * 56 * 896;
#pragma unroll
  for (int j = 0; j < 4; ++j) glds(l + j * 1024, L.v2, L.r2, s + j * 7168);
  glds(l + 4096, L.v2, L.ro, so);
  glds(l + 5120, L.v2, L.ro, so + 7168);
}
DI void issue_w2_g5(const Ld& L) {
  unsigned l = 2 * STG + L.w * 6144, so = L.w * 56 * 896;
  glds(l, L.v2, L.ro, so + 2 * 7168);
  glds(l + 1024, L.v2b, L.ro, so + 3 * 7168);
  so += 28 * 896;
  glds(l + 2048, L.v2, L.ro, so);
  glds(l + 3072, L.v2, L.ro, so + 7168);
  glds(l + 4096, L.v2, L.ro, so + 2 * 7168);
  glds(l + 5120, L.v2b, L.ro, so + 3 * 7168);
}

template <int NB> struct Frag { bf16x8 x; bf16x8 w[NB]; };
template <int NB, int S> DI void ld_frag(Frag<NB>& f, unsigned sb, const unsigned (&wa)[NB], unsigned xa0, unsigned xa1) {
  f.x = ld128(sb + (S < 2 ? xa0 : xa1) + (S & 1) * 32);
#pragma unroll
  for (int b = 0; b < NB; ++b) f.w[b] = ldtr(sb + wa[b] + S * 8704, 2176);
}
struct G6 { rsrc_t ra, rb; unsigned va, vb, la, lb, sa, sb, dla, dlb, dsb; };
template <int K> DI void g6_issue(const G6& g) {
  if (K == 0) glds(g.la, g.va, g.ra, g.sa);
  else if (K == 3) glds(g.la + g.dla, g.va, g.ra, g.sa + 7168);
  else { constexpr int j = K < 3 ? K - 1 : K - 2; glds(g.lb + j * g.dlb, g.vb, g.rb, g.sb + j * g.dsb); }
}
template <int NB, bool TR, bool SQ, int SN, int G>
DI void step(const Frag<NB>& f, Frag<NB>& n, unsigned sn, const unsigned (&wa)[NB], unsigned xa0, unsigned xa1, const G6& g6,
             f32x16 (&acc)[NB], float (&cs)[NB], float (&st)[2], bool docs) {
  const u32x4 xv = __builtin_bit_cast(u32x4, f.x);
#pragma unroll
  for (int b = 0; b < NB; ++b) {
    acc[b] = TR ? mfma(f.w[b], f.x, acc[b]) : mfma(f.x, f.w[b], acc[b]);
    SCHED();
    if (SN >= 0) {
      if (b == 0) n.x = ld128(sn + (SN < 2 ? xa0 : xa1) + (SN & 1) * 32);
      if (2 * b < NB) n.w[2 * b] = ldtr(sn + wa[2 * b] + SN * 8704, 2176);
      if (2 * b + 1 < NB) n.w[2 * b + 1] = ldtr(sn + wa[2 * b + 1] + SN * 8704, 2176);
    }
    if (b >= NB - 2) {
      constexpr int NB2 = NB - 2;
      st[0] = dot2(xv[2 * (b - NB2)], SQ ? xv[2 * (b - NB2)] : 0x3f803f80u, st[0]);
      st[1] = dot2(xv[2 * (b - NB2) + 1], SQ ? xv[2 * (b - NB2) + 1] : 0x3f803f80u, st[1]);
      asm volatile("" : "+v"(st[0]), "+v"(st[1]));
    }
    if (G >= 0 && b == NB - 2) g6_issue<G < 0 ? 0 : G>(g6);
    if (G >= 0 && b == NB - 1) g6_issue<G < 0 ? 0 : G + 1>(g6);
    SCHED();
  }
  if (docs) {
#pragma unroll
    for (int j = 0; j < 4; ++j)
#pragma unroll
      for (int b = 0; b < NB; ++b) cs[b] = dot2(__builtin_bit_cast(u32x4, f.w[b])[j], 0x3f803f80u, cs[b]);
  }
  SCHED();
}
template <int NB, bool TR, bool SQ>
DI void kloop(f32x16 (&acc)[NB], const unsigned (&wa)[NB], unsigned xa0, unsigned xa1, const Ld& L, unsigned b0, unsigned lane) {
  float cs[NB]; float st[2] = {0.f, 0.f};
  const unsigned g = L.w & 3;
#pragma unroll
  for (int b = 0; b < NB; ++b) { cs[b] = 0.f; for (int r = 0; r < 16; ++r) acc[b][r] = 0.f; }
  Frag<NB> f0, f1;
  unsigned sb = 2 * STG, fb = STG;
  ld_frag<NB, 0>(f0, sb, wa, xa0, xa1);
#pragma unroll 1
  for (unsigned i = 0; i < 7; ++i) {
    G6 g6;
    {
      const bool k = i < 5;
      unsigned c = L.c0 + i + 2; c = c >= 7 ? c - 7 : c;
      const unsigned s2 = (L.w * 224 + (i - 5) * 48) * 896, l2 = fb + L.w * 6144;
      g6.ra = k ? L.rx : L.r2; g6.rb = k ? L.rw : L.r2;
      g6.va = k ? L.vx : L.v2; g6.vb = k ? L.vw : L.v2;
      g6.la = k ? fb + XOFF + L.w * 2080 : l2; g6.lb = k ? fb + L.w * 4352 : l2 + 2048;
      g6.sa = k ? c * 128 : s2; g6.sb = k ? c * 64 * L.rs : s2 + 2 * 7168;
      g6.dla = k ? 1040 : 1024; g6.dlb = k ? 1088 : 1024; g6.dsb = k ? 2 * L.rs : 7168;
    }
    const unsigned nb = sb == 2 * STG ? 0 : sb + STG;
    SCHED();
    step<NB, TR, SQ, 1, 0>(f0, f1, sb, wa, xa0, xa1, g6, acc, cs, st, g == 0);
    step<NB, TR, SQ, 2, 2>(f1, f0, sb, wa, xa0, xa1, g6, acc, cs, st, g == 1);
    step<NB, TR, SQ, 3, 4>(f0, f1, sb, wa, xa0, xa1, g6, acc, cs, st, g == 2);
    __builtin_amdgcn_s_waitcnt(0xC07F);
    WAITV(6);
    BAR();
    SCHED();
    step<NB, TR, SQ, 0, -1>(f1, f0, nb, wa, xa0, xa1, g6, acc, cs, st, g == 3);
    fb = sb; sb = nb;
  }
  SCHED();
  *(LAS float*)(L_ST + (SQ ? 512 : 0) + (g * 32 + (lane & 31)) * 4) = halfsum(st[0] + st[1]);
#pragma unroll
  for (int b = 0; b < NB; ++b) *(LAS float*)(L_CSP + (((b0 + b) * 4 + g) * 32 + (lane & 31)) * 4) = halfsum(cs[b]);
  WAITL0();
  BAR();
}
DI float colsum(unsigned blk, unsigned l32) {
  unsigned a = L_CSP + (blk * 128 + l32) * 4;
  return (*(LAS float*)a + *(LAS float*)(a + 128)) + (*(LAS float*)(a + 256) + *(LAS float*)(a + 384));
}
DI void rowstat(unsigned tok, float& mu, float& var) {
  float s1 = *(LAS float*)(L_ST + tok * 4), s2 = *(LAS float*)(L_ST + 512 + tok * 4);
  mu = s1 * (1.f / 448.f);
  var = fmaxf(s2 * (1.f / 448.f) - mu * mu, 0.f);
}
template <int N0, int N1> DI void w2_to_regs(bf16x8 (&w2r)[9][2], unsigned a20, unsigned a21) {
#pragma unroll
  for (int n = N0; n < N1; ++n) {
    w2r[n][0] = ldtr(a20 + (n / 3) * STG + (n % 3) * 2048, 1024);
    w2r[n][1] = ldtr(a21 + (n / 3) * STG + (n % 3) * 2048, 1024);
  }
}

template <bool GELU> DI void epi_tr(const f32x16& acc, unsigned blk, float a, float m, unsigned hh, rsrc_t rws, unsigned voff, unsigned soff) {
  float v[16];
#pragma unroll
  for (int q = 0; q < 4; ++q) {
    f32x4 c = ldf4(L_CSF + blk * 256 + q * 32 + hh * 16);
    f32x4 b = {0.f, 0.f, 0.f, 0.f};
    if (GELU) b = ldf4(L_CSF + blk * 256 + 128 + q * 32 + hh * 16);
#pragma unroll
    for (int j = 0; j < 4; ++j) {
      float u = fmaf(a, acc[q * 4 + j], fmaf(-m, c[j], b[j]));
      if (GELU) {
        float p = fmaf(u * u, -0.10294323970248637f, -2.302208198144325f);
        float e = __builtin_amdgcn_exp2f(u * p);
        v[q * 4 + j] = u * __builtin_amdgcn_rcpf(1.f + e);
      } else {
        float t = phi(u);
        v[q * 4 + j] = (q == 3 && hh) ? 0.f : t;
      }
    }
  }
  u32x4 p0 = {pk2(v[0], v[1]), pk2(v[2], v[3]), pk2(v[4], v[5]), pk2(v[6], v[7])};
  u32x4 p1 = {pk2(v[8], v[9]), pk2(v[10], v[11]), pk2(v[12], v[13]), pk2(v[14], v[15])};
  stsc(p0, rws, voff, soff);
  stsc(p1, rws, voff, soff + 1024);
}

template <int NB, bool SQ>
DI void unit_w1(const Ld& L, unsigned o0, unsigned b0, const float (&bias)[4], unsigned xa0, unsigned xa1, unsigned wl, unsigned par, unsigned lane,
                rsrc_t rws, unsigned hsoff, bf16x8 (&w2r)[9][2], unsigned a20, unsigned a21, u64 (&sv)[16]) {
  f32x16 acc[NB]; unsigned wa[NB];
#pragma unroll
  for (int b = 0; b < NB; ++b) wa[b] = wl + ((o0 + (b0 + b) * 64) ^ par);
  kloop<NB, true, SQ>(acc, wa, xa0, xa1, L, b0, lane);
  STAMP(1);
  issue_w2_h(L, 2, 2);
  const unsigned l32 = lane & 31, hh = lane >> 5;
#pragma unroll
  for (int b = 0; b < NB; ++b) {
    float c = colsum(b0 + b, l32);
    *(LAS float*)(L_CSF + (b0 + b) * 256 + lane * 4) = hh ? bias[b] : c;
  }
  float mu, var; rowstat((L.w & 3) * 32 + l32, mu, var);
  float r = __builtin_amdgcn_rsqf(var + 1e-5f);
  float a = r * __builtin_amdgcn_rsqf(var * r * r + 1e-5f), m = a * mu;
  WAITV(6);
  w2_to_regs<0, 6>(w2r, a20, a21);
  WAITL0();
  SCHED();
  STAMP(12);
  issue_w2_h(L, 3, 0);
  issue_w2_g4(L);
  STAMP(13);
#pragma unroll
  for (int b = 0; b < NB - 1; ++b) { epi_tr<true>(acc[b], b0 + b, a, m, hh, rws, lane * 16, hsoff + (b0 + b) * 2048); SCHED(); }
  if (NB == 4) WAITV(18); else WAITV(16);
  w2_to_regs<6, 9>(w2r, a20, a21);
  epi_tr<true>(acc[NB - 1], b0 + NB - 1, a, m, hh, rws, lane * 16, hsoff + (b0 + NB - 1) * 2048);
  WAITL0();
  SCHED();
  issue_w2_g5(L);
  STAMP(8);
  WAITV(6);
}
template <bool SQ>
DI void unit_q(const Ld& L, unsigned o0, unsigned b0, unsigned xa0, unsigned xa1, unsigned wl, unsigned par, unsigned lane, rsrc_t rws, unsigned qsoff, u64 (&sv)[16]) {
  f32x16 acc[4]; unsigned wa[4];
#pragma unroll
  for (int b = 0; b < 4; ++b) wa[b] = wl + ((o0 + (b0 + b) * 56) ^ par);
  kloop<4, true, SQ>(acc, wa, xa0, xa1, L, b0, lane);
  STAMP(1);
  const unsigned l32 = lane & 31, hh = lane >> 5;
#pragma unroll
  for (int b = 0; b < 4; ++b) {
    float c = colsum(b0 + b, l32);
    *(LAS float*)(L_CSF + (b0 + b) * 256 + lane * 4) = c;
  }
  float mu, var; rowstat((L.w & 3) * 32 + l32, mu, var);
  float a = __builtin_amdgcn_rsqf(var + 1e-5f), m = a * mu;
#pragma unroll
  for (int b = 0; b < 4; ++b) epi_tr<false>(acc[b], b0 + b, a, m, hh, rws, lane * 16, qsoff + (b0 + b) * 2048);
  STAMP(8);
  WAITV(0);
}
template <int NH, bool SQ>
DI void unit_kv(const Ld& L, unsigned o0, unsigned h0, unsigned xa0, unsigned xa1, unsigned wl, unsigned par, unsigned lane,
                bf16x8 (&w2r)[9][2], unsigned a20, unsigned a21, u64 (&sv)[16]) {
  f32x16 acc[2 * NH]; unsigned wa[2 * NH];
#pragma unroll
  for (int b = 0; b < 2 * NH; ++b) wa[b] = wl + ((o0 + (h0 + (b >> 1)) * 56 + (b & 1) * 256) ^ par);
  kloop<2 * NH, false, SQ>(acc, wa, xa0, xa1, L, 2 * h0, lane);
  STAMP(1);
  const unsigned l32 = lane & 31, hh = lane >> 5, g = L.w & 3;
  float mu, var; rowstat(g * 32 + l32, mu, var);
  float r = __builtin_amdgcn_rsqf(var + 1e-5f);
  *(LAS float*)(L_RS + hh * 512 + (g * 32 + l32) * 4) = hh ? r * mu : r;
  float cs[2 * NH];
#pragma unroll
  for (int b = 0; b < 2 * NH; ++b) cs[b] = colsum(2 * h0 + b, l32);
  WAITV(0);
  w2_to_regs<0, 6>(w2r, a20, a21);
  WAITL0();
  SCHED();
  issue_w2_h(L, 3, 0);
  issue_w2_g4(L);
  f32x4 ra[4], rm[4];
#pragma unroll
  for (int q = 0; q < 4; ++q) { ra[q] = ldf4(L_RS + (g * 32 + q * 8 + hh * 4) * 4); rm[q] = ldf4(L_RS + 512 + (g * 32 + q * 8 + hh * 4) * 4); }
  const float vpad = l32 == 28 ? 1.f : 0.f;
#pragma unroll
  for (int h = 0; h < NH; ++h) {
    float k[16], v[16];
#pragma unroll
    for (int i = 0; i < 16; ++i) {
      float kk = phi(fmaf(ra[i >> 2][i & 3], acc[2 * h][i], -rm[i >> 2][i & 3] * cs[2 * h]));
      float vv = fmaf(ra[i >> 2][i & 3], acc[2 * h + 1][i], -rm[i >> 2][i & 3] * cs[2 * h + 1]);
      k[i] = l32 < 28 ? kk : 0.f;
      v[i] = l32 < 28 ? vv : vpad;
    }
    f32x16 kv;
#pragma unroll
    for (int i = 0; i < 16; ++i) kv[i] = 0.f;
#pragma unroll
    for (int s = 0; s < 2; ++s) {
      u32x4 pk = {pk2(k[8 * s], k[8 * s + 1]), pk2(k[8 * s + 2], k[8 * s + 3]), pk2(k[8 * s + 4], k[8 * s + 5]), pk2(k[8 * s + 6], k[8 * s + 7])};
      u32x4 pv = {pk2(v[8 * s], v[8 * s + 1]), pk2(v[8 * s + 2], v[8 * s + 3]), pk2(v[8 * s + 4], v[8 * s + 5]), pk2(v[8 * s + 6], v[8 * s + 7])};
      kv = mfma(__builtin_bit_cast(bf16x8, pk), __builtin_bit_cast(bf16x8, pv), kv);
    }
#pragma unroll
    for (int q = 0; q < 4; ++q) {
      f32x4 t = {kv[4 * q], kv[4 * q + 1], kv[4 * q + 2], kv[4 * q + 3]};
      *(LAS f32x4*)(2 * STG + (((h0 + h) * 4 + g) * 4 + q) * 1024 + lane * 16) = t;
    }
  }
  WAITL0();
}

__global__ __launch_bounds__(512, 2) void fused_kernel(const char* __restrict__ x, const char* __restrict__ wqkv, const char* __restrict__ wo,
                                                       const char* __restrict__ w1, const char* __restrict__ b1, const char* __restrict__ w2,
                                                       const char* __restrict__ b2, char* __restrict__ out, char* ws) {
  extern __shared__ __attribute__((aligned(16))) char lds_dyn[];
  const unsigned tid = threadIdx.x, lane = tid & 63, w = __builtin_amdgcn_readfirstlane(tid >> 6);
  const unsigned xcd = blockIdx.x & 7, bi = blockIdx.x >> 3;
  u64 sv[16] = {0, 0, 0, 0, 0, 0, 0, 0, 0, 0, 0, 0, 0, 0, 0, 0};
  CYC(14);
  STAMP(0);
  const unsigned panel = (xcd & 3) * 4 + (bi >> 3), j = bi & 7;
  const bool is_w1 = xcd < 4, is_q = !is_w1 && j >= 6;
  const unsigned slot = is_w1 ? j : 8 + j;
  unsigned u2 = is_w1 ? xcd * 32 + bi : 128 + (xcd - 4) * 24 + (bi >> 3) * 6 + j;
  if (is_q) u2 = 0;
  const unsigned T = u2 / 7, ct = u2 - T * 7;
  unsigned seg0, seg1, o0;
  const unsigned kvh0 = j < 2 ? 3 * j : (j < 4 ? 2 * j + 2 : 3 * j - 2);
  if (is_w1) { seg0 = (448 * j) & ~127u; seg1 = seg0 + 256; o0 = (j & 1) * 64; }
  else if (is_q) { seg0 = (j - 6) * 384; seg1 = seg0 + 256; o0 = (j - 6) * 64; }
  else { seg0 = 896 + 128 * j; seg1 = 1792 + 128 * j; o0 = 56 * kvh0 - 128 * j; }

  Ld L;
  L.w = w;
  L.rs = is_w1 ? 3584 : 2688;
#define ROT 0
  L.c0 = ROT == 1 ? (panel * 2 + j) % 7 : (ROT == 2 ? (panel & 2) * 2 : (ROT == 3 ? ((panel & 3) * 7) / 4 : 0));
  L.rx = __builtin_amdgcn_make_buffer_rsrc((void*)(x + panel * 128 * 896), 0, 128 * 896, 0x00020000);
  L.rw = __builtin_amdgcn_make_buffer_rsrc((void*)(is_w1 ? w1 : wqkv), 0, 448 * L.rs, 0x00020000);
  L.r2 = __builtin_amdgcn_make_buffer_rsrc((void*)w2, 0, 1792 * 896, 0x00020000);
  L.ro = __builtin_amdgcn_make_buffer_rsrc((void*)wo, 0, 448 * 896, 0x00020000);
  const rsrc_t rws = __builtin_amdgcn_make_buffer_rsrc((void*)ws, 0, W_END, 0x00020000);
  {
    unsigned r8 = lane >> 3, c = ((lane & 7) ^ ((lane >> 2) & 4)) * 16;
    L.vx = (w * 16 + r8) * 896 + c;
    L.v2 = r8 * 896 + c + ct * 128;
    L.v2b = (r8 & 3) * 896 + c + ct * 128;
    unsigned rp = lane >> 5, sp = ((lane & 31) * 16) ^ (rp * 128);
    L.vw = (w * 8 + rp) * L.rs + ((sp & 256) ? seg1 : seg0) + (sp & 255);
  }
  const unsigned grp = w >> 2, b0w = grp * 4;
  u32x2 gen; unsigned bb[4];
  {
    unsigned zero = 0, bo = is_w1 ? (224 * j + b0w * 32 + (lane & 31)) * 2 : 0;
    unsigned bo3 = grp ? bo : bo + 192;
    asm volatile("s_nop 4\n\tglobal_load_dwordx2 %0, %5, %6 sc1\n\tglobal_load_ushort %1, %7, %8\n\tglobal_load_ushort %2, %7, %8 offset:64\n\t"
                 "global_load_ushort %3, %7, %8 offset:128\n\tglobal_load_ushort %4, %9, %8"
                 : "=&v"(gen), "=&v"(bb[0]), "=&v"(bb[1]), "=&v"(bb[2]), "=&v"(bb[3]) : "v"(zero), "s"(ws), "v"(bo), "s"(b1), "v"(bo3) : "memory");
  }
  issue_chunk(L, 0, 2 * STG);
  issue_chunk(L, 1, 0);
  const unsigned l32 = lane & 31, hh = lane >> 5, q4 = (lane & 15) >> 2, blk = (lane >> 4) & 1, p4 = lane & 3;
  unsigned xa0, xa1;
  {
    unsigned r = (w & 3) * 32 + l32, f = (r >> 1) & 1, base = XOFF + (r >> 3) * 1040 + (r & 7) * 128 + hh * 16;
    xa0 = base + f * 64; xa1 = base + (f ^ 1) * 64;
  }
  const unsigned par = (q4 & 1) * 128, wl = 1088 * (4 * hh + (q4 >> 1)) + (q4 & 1) * 512;
  o0 += blk * 32 + p4 * 8;
  const unsigned a20 = (4 * hh + q4) * 128 + (q4 >> 1) * 64 + blk * 32 + p4 * 8 + w * 6144, a21 = a20 ^ 64;
  asm volatile("s_waitcnt vmcnt(6)" : "+v"(gen), "+v"(bb[0]), "+v"(bb[1]), "+v"(bb[2]), "+v"(bb[3])::"memory");
  const u64 g0 = ((u64)(unsigned)__builtin_amdgcn_readfirstlane(gen[1]) << 32) | (unsigned)__builtin_amdgcn_readfirstlane(gen[0]);
  const u64 tag = g0 * 6364136223846793005ull + 1442695040888963407ull;
  BAR();
  STAMP(7);

  bf16x8 w2r[9][2];
  if (is_w1) {
    float bias[4];
#pragma unroll
    for (int b = 0; b < 4; ++b) bias[b] = __uint_as_float(bb[b] << 16);
    unsigned hsoff = W_H + ((panel * 4 + (w & 3)) * 112 + j * 14) * 1024;
    if (grp == 0) unit_w1<4, false>(L, o0, 0, bias, xa0, xa1, wl, par, lane, rws, hsoff, w2r, a20, a21, sv);
    else unit_w1<3, true>(L, o0, 4, bias, xa0, xa1, wl, par, lane, rws, hsoff, w2r, a20, a21, sv);
    BAR();
    if (tid == 0) __hip_atomic_store((GAS u64*)(ws + W_F1 + (panel * 16 + slot) * 128), tag, __ATOMIC_RELAXED, __HIP_MEMORY_SCOPE_AGENT);
  } else if (is_q) {
    unsigned qsoff = W_PQ + ((panel * 4 + (w & 3)) * 16 + (j - 6) * 8) * 2048;
    if (grp == 0) unit_q<false>(L, o0, 0, xa0, xa1, wl, par, lane, rws, qsoff, sv);
    else unit_q<true>(L, o0, 4, xa0, xa1, wl, par, lane, rws, qsoff, sv);
    BAR();
    if (tid == 0) __hip_atomic_store((GAS u64*)(ws + W_F1 + (panel * 16 + slot) * 128), tag, __ATOMIC_RELAXED, __HIP_MEMORY_SCOPE_AGENT);
    STAMP(2);
    const unsigned rj = panel * 2 + (j - 6), head = rj >> 1, sh = rj & 1;
    if (w == 0) {
      const unsigned gj = head < 6 ? head / 3 : (head < 10 ? (head - 2) >> 1 : (head + 2) / 3);
      wait_flag(ws, W_F1 + ((lane & 15) * 16 + 8 + gj) * 128, tag);
      STAMP(3);
      u32x4 t[16][2];
#pragma unroll
      for (int p = 0; p < 16; ++p) {
        t[p][0] = ldsc(rws, lane * 16, W_KVP + (p * 16 + head) * 4096 + sh * 2048);
        t[p][1] = ldsc(rws, lane * 16, W_KVP + (p * 16 + head) * 4096 + sh * 2048 + 1024);
      }
      f32x4 s0 = __builtin_bit_cast(f32x4, t[0][0]), s1 = __builtin_bit_cast(f32x4, t[0][1]);
#pragma unroll
      for (int p = 1; p < 16; ++p) { s0 += __builtin_bit_cast(f32x4, t[p][0]); s1 += __builtin_bit_cast(f32x4, t[p][1]); }
      u32x4 o = {pk2(s0[0], s0[1]), pk2(s0[2], s0[3]), pk2(s1[0], s1[1]), pk2(s1[2], s1[3])};
      stsc(o, rws, lane * 16, W_KVF + rj * 1024);
      WAITV(0);
      if (lane == 0) __hip_atomic_store((GAS u64*)(ws + W_FK + rj * 128), tag, __ATOMIC_RELAXED, __HIP_MEMORY_SCOPE_AGENT);
      STAMP(4);
    } else if (w == 1 && rj == 0) {
#pragma unroll 1
      for (int k = 0; k < 4; ++k) wait_flag(ws, W_F1 + (k * 64 + lane) * 128, tag);
      if (lane == 0) __hip_atomic_store((GAS u64*)(ws + W_GEN), tag, __ATOMIC_RELAXED, __HIP_MEMORY_SCOPE_AGENT);
    }
    STAMP(6); STAMP_OUT();
    return;
  } else {
    const bool three = j < 2 || j >= 4;
    if (grp == 0) {
      if (three) unit_kv<2, false>(L, o0, 0, xa0, xa1, wl, par, lane, w2r, a20, a21, sv);
      else unit_kv<1, false>(L, o0, 0, xa0, xa1, wl, par, lane, w2r, a20, a21, sv);
    } else unit_kv<1, true>(L, o0, three ? 2 : 1, xa0, xa1, wl, par, lane, w2r, a20, a21, sv);
    BAR();
    for (unsigned it = w; it < (three ? 12u : 8u); it += 8) {
      unsigned a = 2 * STG + ((it >> 2) * 16 + (it & 3)) * 1024 + lane * 16;
      f32x4 s = (ldf4(a) + ldf4(a + 4096)) + (ldf4(a + 8192) + ldf4(a + 12288));
      stsc(__builtin_bit_cast(u32x4, s), rws, lane * 16, W_KVP + (panel * 16 + kvh0) * 4096 + it * 1024);
    }
    WAITV(0);
    BAR();
    if (tid == 0) __hip_atomic_store((GAS u64*)(ws + W_F1 + (panel * 16 + slot) * 128), tag, __ATOMIC_RELAXED, __HIP_MEMORY_SCOPE_AGENT);
    issue_w2_h(L, 2, 2);
    WAITV(0);
    w2_to_regs<6, 9>(w2r, a20, a21);
    WAITL0();
    SCHED();
    issue_w2_g5(L);
  }
  STAMP(2);

  const unsigned p2 = T >> 1;
  const unsigned otok = T * 64 + (tid >> 3), ocol = ct * 64 + (tid & 7) * 8;
  const u32x4 xin = *(const GAS u32x4*)(x + (otok * 448 + ocol) * 2);
  const u32x4 bin = *(const GAS u32x4*)(b2 + ocol * 2);
  wait_flag(ws, W_F1 + (p2 * 16 + w) * 128, tag);
  STAMP(3);
  f32x16 acc[2][2];
#pragma unroll
  for (int i = 0; i < 4; ++i)
#pragma unroll
    for (int r = 0; r < 16; ++r) acc[i >> 1][i & 1][r] = 0.f;
  {
    const unsigned hb = W_H + (T * 2 * 112 + w * 14) * 1024;
    u32x4 h[14][2];
#pragma unroll
    for (int n = 0; n < 8; ++n) { h[n][0] = ldsc(rws, lane * 16, hb + n * 1024); h[n][1] = ldsc(rws, lane * 16, hb + (112 + n) * 1024); }
    SCHED();
#pragma unroll
    for (int n = 0; n < 14; ++n) {
      bf16x8 wa, wb;
      if (n < 9) { wa = w2r[n][0]; wb = w2r[n][1]; }
      else { wa = ldtr(a20 + ((n - 9) / 3) * STG + ((n - 9) % 3) * 2048, 1024); wb = ldtr(a21 + ((n - 9) / 3) * STG + ((n - 9) % 3) * 2048, 1024); }
      bf16x8 h0 = __builtin_bit_cast(bf16x8, h[n][0]), h1 = __builtin_bit_cast(bf16x8, h[n][1]);
      acc[0][0] = mfma(wa, h0, acc[0][0]);
      acc[0][1] = mfma(wb, h0, acc[0][1]);
      acc[1][0] = mfma(wa, h1, acc[1][0]);
      acc[1][1] = mfma(wb, h1, acc[1][1]);
      if (n + 8 < 14) { h[n + 8][0] = ldsc(rws, lane * 16, hb + (n + 8) * 1024); h[n + 8][1] = ldsc(rws, lane * 16, hb + (112 + n + 8) * 1024); }
      SCHED();
    }
  }
  STAMP(4);
  {
    unsigned fo = lane < 4 ? W_FK + (w * 4 + lane) * 128 : W_F1 + (p2 * 16 + 14 + (w >> 2)) * 128;
    wait_flag(ws, fo, tag);
    STAMP(5);
    u32x4 kf[2][2], pq[2][2][2];
#pragma unroll
    for (int hd = 0; hd < 2; ++hd)
#pragma unroll
      for (int s = 0; s < 2; ++s) {
        kf[hd][s] = ldsc(rws, lane * 16, W_KVF + ((w * 2 + hd) * 2 + s) * 1024);
        pq[hd][0][s] = ldsc(rws, lane * 16, W_PQ + (((T * 2) * 16 + w * 2 + hd) * 2 + s) * 1024);
        pq[hd][1][s] = ldsc(rws, lane * 16, W_PQ + (((T * 2 + 1) * 16 + w * 2 + hd) * 2 + s) * 1024);
      }
#pragma unroll
    for (int hd = 0; hd < 2; ++hd) {
      bf16x8 wf[2][2];
#pragma unroll
      for (int s = 0; s < 2; ++s) {
        const int n = 5 + hd * 2 + s;
        wf[s][0] = ldtr(a20 + (n / 3) * STG + (n % 3) * 2048, 1024);
        wf[s][1] = ldtr(a21 + (n / 3) * STG + (n % 3) * 2048, 1024);
      }
#pragma unroll
      for (int th = 0; th < 2; ++th) {
        f32x16 nm;
#pragma unroll
        for (int r = 0; r < 16; ++r) nm[r] = 0.f;
        nm = mfma(__builtin_bit_cast(bf16x8, kf[hd][0]), __builtin_bit_cast(bf16x8, pq[hd][th][0]), nm);
        nm = mfma(__builtin_bit_cast(bf16x8, kf[hd][1]), __builtin_bit_cast(bf16x8, pq[hd][th][1]), nm);
        float rd = __builtin_amdgcn_rcpf(bcast_hi(nm[12]) + 1e-6f);
        float o[16];
#pragma unroll
        for (int r = 0; r < 16; ++r) o[r] = (r >= 12 && hh) ? 0.f : nm[r] * rd;
#pragma unroll
        for (int s = 0; s < 2; ++s) {
          u32x4 po = {pk2(o[8 * s], o[8 * s + 1]), pk2(o[8 * s + 2], o[8 * s + 3]), pk2(o[8 * s + 4], o[8 * s + 5]), pk2(o[8 * s + 6], o[8 * s + 7])};
          acc[th][0] = mfma(wf[s][0], __builtin_bit_cast(bf16x8, po), acc[th][0]);
          acc[th][1] = mfma(wf[s][1], __builtin_bit_cast(bf16x8, po), acc[th][1]);
        }
      }
    }
  }
  STAMP(9);
  WAITL0();
  BAR();
  STAMP(10);
#pragma unroll
  for (int th = 0; th < 2; ++th)
#pragma unroll
    for (int cb = 0; cb < 2; ++cb)
#pragma unroll
      for (int q = 0; q < 4; ++q) {
        f32x4 t = {acc[th][cb][4 * q], acc[th][cb][4 * q + 1], acc[th][cb][4 * q + 2], acc[th][cb][4 * q + 3]};
        *(LAS f32x4*)(w * 17408 + (th * 32 + l32) * 272 + (cb * 32 + q * 8 + hh * 4) * 4) = t;
      }
  WAITL0();
  BAR();
  STAMP(11);
  {
    unsigned a = (tid >> 3) * 272 + (tid & 7) * 32;
    f32x4 s0 = ldf4(a), s1 = ldf4(a + 16);
#pragma unroll
    for (int p = 1; p < 8; ++p) { s0 += ldf4(a + p * 17408); s1 += ldf4(a + p * 17408 + 16); }
    float r[8];
#pragma unroll
    for (int i = 0; i < 4; ++i) {
      float lo = __uint_as_float(xin[i] << 16) + __uint_as_float(bin[i] << 16), hi = __uint_as_float(xin[i] & 0xffff0000u) + __uint_as_float(bin[i] & 0xffff0000u);
      r[2 * i] = lo + (i < 2 ? s0[2 * i] : s1[2 * i - 4]);
      r[2 * i + 1] = hi + (i < 2 ? s0[2 * i + 1] : s1[2 * i - 3]);
    }
    u32x4 o = {pk2(r[0], r[1]), pk2(r[2], r[3]), pk2(r[4], r[5]), pk2(r[6], r[7])};
    *(GAS u32x4*)(out + (otok * 448 + ocol) * 2) = o;
  }
  STAMP(6); STAMP_OUT();
}

extern "C" void kernel_launch(void* const* d_in, const int* in_sizes, int n_in, void* d_out, int out_size, void* d_ws, size_t ws_size,
                              hipStream_t stream) {
  (void)hipFuncSetAttribute((const void*)fused_kernel, hipFuncAttributeMaxDynamicSharedMemorySize, LDS_BYTES);
  fused_kernel<<<256, 512, LDS_BYTES, stream>>>((const char*)d_in[0], (const char*)d_in[1], (const char*)d_in[2], (const char*)d_in[4],
                                                (const char*)d_in[5], (const char*)d_in[6], (const char*)d_in[7], (char*)d_out, (char*)d_ws);
}
```

```cpp
#define ROLE(b) (((b)&7)<4?0:((((b)>>3)&7)>=6?2:1))
#define STAMPS
#include <cstdio>
#include <hip/hip_runtime.h>
#pragma clang diagnostic ignored "-Wint-to-pointer-cast"

typedef unsigned long long u64;
typedef __attribute__((ext_vector_type(8))) __bf16 bf16x8;
typedef __attribute__((ext_vector_type(2))) __bf16 bf16x2;
typedef __attribute__((ext_vector_type(4))) short s16x4;
typedef __attribute__((ext_vector_type(16))) float f32x16;
typedef __attribute__((ext_vector_type(4))) float f32x4;
typedef __attribute__((ext_vector_type(2))) float f32x2;
typedef __attribute__((ext_vector_type(4))) unsigned u32x4;
typedef __attribute__((ext_vector_type(2))) unsigned u32x2;
typedef __amdgpu_buffer_rsrc_t rsrc_t;
#define GAS __attribute__((address_space(1)))
#define LAS __attribute__((address_space(3)))
#define DI __device__ __forceinline__
#define SCHED() __builtin_amdgcn_sched_barrier(0)
#define BAR() __builtin_amdgcn_s_barrier()
#define WAITV(n) asm volatile("s_waitcnt vmcnt(" #n ")" ::: "memory")
#define WAITL0() asm volatile("s_waitcnt lgkmcnt(0)" ::: "memory")

constexpr unsigned STG = 51456;
constexpr unsigned XOFF = 34816;
constexpr unsigned L_CSP = 154368;
constexpr unsigned L_ST = 158464;
constexpr unsigned L_CSF = 159488;
constexpr unsigned L_RS = 161536;
constexpr unsigned LDS_BYTES = 163840;
constexpr unsigned W_GEN = 0;
constexpr unsigned W_F1 = 4096;
constexpr unsigned W_FK = W_F1 + 256 * 128;
constexpr unsigned W_KVF = 65536;
constexpr unsigned W_KVP = 131072;
constexpr unsigned W_PQ = W_KVP + (1u << 20);
constexpr unsigned W_H = W_PQ + (2u << 20);
constexpr unsigned W_END = W_H + 64u * 112u * 1024u;
constexpr unsigned W_DBG = 16u << 20;
constexpr int SPIN_MAX = 400000;

#define STAMP(i) do { SCHED(); sv[i] = __builtin_amdgcn_s_memrealtime(); SCHED(); } while (0)
#define CYC(i) do { sv[i] = __builtin_amdgcn_s_memtime(); } while (0)
#define STAMP_OUT() do { CYC(15); if ((threadIdx.x & 63) == 0) for (int i_ = 0; i_ < 16; ++i_) ((GAS u64*)(ws + W_DBG))[(blockIdx.x * 8 + (threadIdx.x >> 6)) * 16 + i_] = sv[i_]; } while (0)

DI void glds(unsigned ldsaddr, unsigned voff, rsrc_t r, unsigned soff) {
  asm volatile("s_mov_b32 m0, %0\n\ts_nop 0\n\tbuffer_load_dwordx4 %1, %2, %3 offen lds" ::"s"(ldsaddr), "v"(voff), "s"(r), "s"(soff) : "memory");
}
DI bf16x8 ld128(unsigned a) { return *(LAS bf16x8*)a; }
DI f32x4 ldf4(unsigned a) { return *(LAS f32x4*)a; }
DI bf16x8 ldtr(unsigned a, unsigned hi) {
  s16x4 lo = __builtin_amdgcn_ds_read_tr16_b64_v4i16((LAS s16x4*)a);
  s16x4 h = __builtin_amdgcn_ds_read_tr16_b64_v4i16((LAS s16x4*)(a + hi));
  return __builtin_bit_cast(bf16x8, __builtin_shufflevector(lo, h, 0, 1, 2, 3, 4, 5, 6, 7));
}
DI f32x16 mfma(bf16x8 a, bf16x8 b, f32x16 c) { return __builtin_amdgcn_mfma_f32_32x32x16_bf16(a, b, c, 0, 0, 0); }
DI float dot2(unsigned a, unsigned b, float c) { return __builtin_amdgcn_fdot2_f32_bf16(__builtin_bit_cast(bf16x2, a), __builtin_bit_cast(bf16x2, b), c, false); }
DI unsigned pk2(float a, float b) { f32x2 v = {a, b}; return __builtin_bit_cast(unsigned, __builtin_convertvector(v, bf16x2)); }
DI float halfsum(float x) { auto t = __builtin_amdgcn_permlane32_swap(__float_as_uint(x), __float_as_uint(x), false, false); return __uint_as_float(t[0]) + __uint_as_float(t[1]); }
DI float bcast_hi(float x) { auto t = __builtin_amdgcn_permlane32_swap(__float_as_uint(x), __float_as_uint(x), false, false); return __uint_as_float(t[1]); }
DI u32x4 ldsc(rsrc_t r, unsigned voff, unsigned soff) { return __builtin_amdgcn_raw_buffer_load_b128(r, voff, soff, 16); }
DI void stsc(u32x4 v, rsrc_t r, unsigned voff, unsigned soff) { __builtin_amdgcn_raw_buffer_store_b128(v, r, voff, soff, 16); }
DI float phi(float t) { return (t > 0.f ? t + 1.f : __builtin_amdgcn_exp2f(t * 1.4426950408889634f)) + 1e-6f; }
DI void wait_flag(const char* ws, unsigned off, u64 tag) {
  for (int s = 0; s < SPIN_MAX; ++s) {
    u64 v = __hip_atomic_load((GAS u64*)(ws + off), __ATOMIC_RELAXED, __HIP_MEMORY_SCOPE_AGENT);
    if (__all(v == tag)) break;
    __builtin_amdgcn_s_sleep(1);
  }
  __builtin_amdgcn_fence(__ATOMIC_ACQUIRE, "wavefront");
}

struct Ld {
  rsrc_t rx, rw, r2, ro;
  unsigned vx, vw, v2, v2b;
  unsigned rs;
  unsigned c0;
  unsigned w;
};
DI void issue_chunk(const Ld& L, unsigned i, unsigned sb) {
  unsigned c = L.c0 + i; c = c >= 7 ? c - 7 : c;
  unsigned sx = c * 128, sw = c * 64 * L.rs;
  unsigned lx = sb + XOFF + L.w * 2080, lw = sb + L.w * 4352;
  glds(lx, L.vx, L.rx, sx);
  glds(lw, L.vw, L.rw, sw);
  glds(lw + 1088, L.vw, L.rw, sw + 2 * L.rs);
  glds(lx + 1040, L.vx, L.rx, sx + 7168);
  glds(lw + 2176, L.vw, L.rw, sw + 4 * L.rs);
  glds(lw + 3264, L.vw, L.rw, sw + 6 * L.rs);
}
DI void issue_w2_h(const Ld& L, unsigned gi, unsigned reg) {
  unsigned l = reg * STG + L.w * 6144, s = (L.w * 224 + gi * 48) * 896;
#pragma unroll
  for (int j = 0; j < 6; ++j) glds(l + j * 1024, L.v2, L.r2, s + j * 7168);
}
DI void issue_w2_g4(const Ld& L) {
  unsigned l = STG + L.w * 6144, s = (L.w * 224 + 192) * 896, so = L.w * 56 * 896;
#pragma unroll
  for (int j = 0; j < 4; ++j) glds(l + j * 1024, L.v2, L.r2, s + j * 7168);
  glds(l + 4096, L.v2, L.ro, so);
  glds(l + 5120, L.v2, L.ro, so + 7168);
}
DI void issue_w2_g5(const Ld& L) {
  unsigned l = 2 * STG + L.w * 6144, so = L.w * 56 * 896;
  glds(l, L.v2, L.ro, so + 2 * 7168);
  glds(l + 1024, L.v2b, L.ro, so + 3 * 7168);
  so += 28 * 896;
  glds(l + 2048, L.v2, L.ro, so);
  glds(l + 3072, L.v2, L.ro, so + 7168);
  glds(l + 4096, L.v2, L.ro, so + 2 * 7168);
  glds(l + 5120, L.v2b, L.ro, so + 3 * 7168);
}

template <int NB> struct Frag { bf16x8 x; bf16x8 w[NB]; };
template <int NB, int S> DI void ld_frag(Frag<NB>& f, unsigned sb, const unsigned (&wa)[NB], unsigned xa0, unsigned xa1) {
  f.x = ld128(sb + (S < 2 ? xa0 : xa1) + (S & 1) * 32);
#pragma unroll
  for (int b = 0; b < NB; ++b) f.w[b] = ldtr(sb + wa[b] + S * 8704, 2176);
}
struct G6 { rsrc_t ra, rb; unsigned va, vb, la, lb, sa, sb, dla, dlb, dsb; };
template <int K> DI void g6_issue(const G6& g) {
  if (K == 0) glds(g.la, g.va, g.ra, g.sa);
  else if (K == 3) glds(g.la + g.dla, g.va, g.ra, g.sa + 7168);
  else { constexpr int j = K < 3 ? K - 1 : K - 2; glds(g.lb + j * g.dlb, g.vb, g.rb, g.sb + j * g.dsb); }
}
template <int NB, bool TR, bool SQ, int SN, int G>
DI void step(const Frag<NB>& f, Frag<NB>& n, unsigned sn, const unsigned (&wa)[NB], unsigned xa0, unsigned xa1, const G6& g6,
             f32x16 (&acc)[NB], float (&cs)[NB], float (&st)[2], bool docs) {
  const u32x4 xv = __builtin_bit_cast(u32x4, f.x);
#pragma unroll
  for (int b = 0; b < NB; ++b) {
    acc[b] = TR ? mfma(f.w[b], f.x, acc[b]) : mfma(f.x, f.w[b], acc[b]);
    SCHED();
    if (SN >= 0) {
      if (b == 0) n.x = ld128(sn + (SN < 2 ? xa0 : xa1) + (SN & 1) * 32);
      if (2 * b < NB) n.w[2 * b] = ldtr(sn + wa[2 * b] + SN * 8704, 2176);
      if (2 * b + 1 < NB) n.w[2 * b + 1] = ldtr(sn + wa[2 * b + 1] + SN * 8704, 2176);
    }
    if (b >= NB - 2) {
      constexpr int NB2 = NB - 2;
      st[0] = dot2(xv[2 * (b - NB2)], SQ ? xv[2 * (b - NB2)] : 0x3f803f80u, st[0]);
      st[1] = dot2(xv[2 * (b - NB2) + 1], SQ ? xv[2 * (b - NB2) + 1] : 0x3f803f80u, st[1]);
      asm volatile("" : "+v"(st[0]), "+v"(st[1]));
    }
    if (G >= 0 && b == NB - 2) g6_issue<G < 0 ? 0 : G>(g6);
    if (G >= 0 && b == NB - 1) g6_issue<G < 0 ? 0 : G + 1>(g6);
    SCHED();
  }
  if (docs) {
#pragma unroll
    for (int j = 0; j < 4; ++j)
#pragma unroll
      for (int b = 0; b < NB; ++b) cs[b] = dot2(__builtin_bit_cast(u32x4, f.w[b])[j], 0x3f803f80u, cs[b]);
  }
  SCHED();
}
template <int NB, bool TR, bool SQ>
DI void kloop(f32x16 (&acc)[NB], const unsigned (&wa)[NB], unsigned xa0, unsigned xa1, const Ld& L, unsigned b0, unsigned lane) {
  float cs[NB]; float st[2] = {0.f, 0.f};
  const unsigned g = L.w & 3;
#pragma unroll
  for (int b = 0; b < NB; ++b) { cs[b] = 0.f; for (int r = 0; r < 16; ++r) acc[b][r] = 0.f; }
  Frag<NB> f0, f1;
  unsigned sb = 2 * STG, fb = STG;
  ld_frag<NB, 0>(f0, sb, wa, xa0, xa1);
#pragma unroll 1
  for (unsigned i = 0; i < 7; ++i) {
    G6 g6;
    {
      const bool k = i < 5;
      unsigned c = L.c0 + i + 2; c = c >= 7 ? c - 7 : c;
      const unsigned s2 = (L.w * 224 + (i - 5) * 48) * 896, l2 = fb + L.w * 6144;
      g6.ra = k ? L.rx : L.r2; g6.rb = k ? L.rw : L.r2;
      g6.va = k ? L.vx : L.v2; g6.vb = k ? L.vw : L.v2;
      g6.la = k ? fb + XOFF + L.w * 2080 : l2; g6.lb = k ? fb + L.w * 4352 : l2 + 2048;
      g6.sa = k ? c * 128 : s2; g6.sb = k ? c * 64 * L.rs : s2 + 2 * 7168;
      g6.dla = k ? 1040 : 1024; g6.dlb = k ? 1088 : 1024; g6.dsb = k ? 2 * L.rs : 7168;
    }
    const unsigned nb = sb == 2 * STG ? 0 : sb + STG;
    SCHED();
    step<NB, TR, SQ, 1, 0>(f0, f1, sb, wa, xa0, xa1, g6, acc, cs, st, g == 0);
    step<NB, TR, SQ, 2, 2>(f1, f0, sb, wa, xa0, xa1, g6, acc, cs, st, g == 1);
    step<NB, TR, SQ, 3, 4>(f0, f1, sb, wa, xa0, xa1, g6, acc, cs, st, g == 2);
    __builtin_amdgcn_s_waitcnt(0xC07F);
    WAITV(6);
    BAR();
    SCHED();
    step<NB, TR, SQ, 0, -1>(f1, f0, nb, wa, xa0, xa1, g6, acc, cs, st, g == 3);
    fb = sb; sb = nb;
  }
  SCHED();
  *(LAS float*)(L_ST + (SQ ? 512 : 0) + (g * 32 + (lane & 31)) * 4) = halfsum(st[0] + st[1]);
#pragma unroll
  for (int b = 0; b < NB; ++b) *(LAS float*)(L_CSP + (((b0 + b) * 4 + g) * 32 + (lane & 31)) * 4) = halfsum(cs[b]);
  WAITL0();
  BAR();
}
DI float colsum(unsigned blk, unsigned l32) {
  unsigned a = L_CSP + (blk * 128 + l32) * 4;
  return (*(LAS float*)a + *(LAS float*)(a + 128)) + (*(LAS float*)(a + 256) + *(LAS float*)(a + 384));
}
DI void rowstat(unsigned tok, float& mu, float& var) {
  float s1 = *(LAS float*)(L_ST + tok * 4), s2 = *(LAS float*)(L_ST + 512 + tok * 4);
  mu = s1 * (1.f / 448.f);
  var = fmaxf(s2 * (1.f / 448.f) - mu * mu, 0.f);
}
template <int N0, int N1> DI void w2_to_regs(bf16x8 (&w2r)[9][2], unsigned a20, unsigned a21) {
#pragma unroll
  for (int n = N0; n < N1; ++n) {
    w2r[n][0] = ldtr(a20 + (n / 3) * STG + (n % 3) * 2048, 1024);
    w2r[n][1] = ldtr(a21 + (n / 3) * STG + (n % 3) * 2048, 1024);
  }
}

template <bool GELU> DI void epi_tr(const f32x16& acc, unsigned blk, float a, float m, unsigned hh, rsrc_t rws, unsigned voff, unsigned soff) {
  float v[16];
#pragma unroll
  for (int q = 0; q < 4; ++q) {
    f32x4 c = ldf4(L_CSF + blk * 256 + q * 32 + hh * 16);
    f32x4 b = {0.f, 0.f, 0.f, 0.f};
    if (GELU) b = ldf4(L_CSF + blk * 256 + 128 + q * 32 + hh * 16);
#pragma unroll
    for (int j = 0; j < 4; ++j) {
      float u = fmaf(a, acc[q * 4 + j], fmaf(-m, c[j], b[j]));
      if (GELU) {
        float p = fmaf(u * u, -0.10294323970248637f, -2.302208198144325f);
        float e = __builtin_amdgcn_exp2f(u * p);
        v[q * 4 + j] = u * __builtin_amdgcn_rcpf(1.f + e);
      } else {
        float t = phi(u);
        v[q * 4 + j] = (q == 3 && hh) ? 0.f : t;
      }
    }
  }
  u32x4 p0 = {pk2(v[0], v[1]), pk2(v[2], v[3]), pk2(v[4], v[5]), pk2(v[6], v[7])};
  u32x4 p1 = {pk2(v[8], v[9]), pk2(v[10], v[11]), pk2(v[12], v[13]), pk2(v[14], v[15])};
  stsc(p0, rws, voff, soff);
  stsc(p1, rws, voff, soff + 1024);
}

template <int NB, bool SQ>
DI void unit_w1(const Ld& L, unsigned o0, unsigned b0, const float (&bias)[4], unsigned xa0, unsigned xa1, unsigned wl, unsigned par, unsigned lane,
                rsrc_t rws, unsigned hsoff, bf16x8 (&w2r)[9][2], unsigned a20, unsigned a21, u64 (&sv)[16]) {
  f32x16 acc[NB]; unsigned wa[NB];
#pragma unroll
  for (int b = 0; b < NB; ++b) wa[b] = wl + ((o0 + (b0 + b) * 64) ^ par);
  kloop<NB, true, SQ>(acc, wa, xa0, xa1, L, b0, lane);
  STAMP(1);
  issue_w2_h(L, 2, 2);
  const unsigned l32 = lane & 31, hh = lane >> 5;
#pragma unroll
  for (int b = 0; b < NB; ++b) {
    float c = colsum(b0 + b, l32);
    *(LAS float*)(L_CSF + (b0 + b) * 256 + lane * 4) = hh ? bias[b] : c;
  }
  float mu, var; rowstat((L.w & 3) * 32 + l32, mu, var);
  float r = __builtin_amdgcn_rsqf(var + 1e-5f);
  float a = r * __builtin_amdgcn_rsqf(var * r * r + 1e-5f), m = a * mu;
  WAITV(6);
  w2_to_regs<0, 6>(w2r, a20, a21);
  WAITL0();
  SCHED();
  STAMP(12);
  issue_w2_h(L, 3, 0);
  issue_w2_g4(L);
  STAMP(13);
#pragma unroll
  for (int b = 0; b < NB - 1; ++b) { epi_tr<true>(acc[b], b0 + b, a, m, hh, rws, lane * 16, hsoff + (b0 + b) * 2048); SCHED(); }
  if (NB == 4) WAITV(18); else WAITV(16);
  w2_to_regs<6, 9>(w2r, a20, a21);
  epi_tr<true>(acc[NB - 1], b0 + NB - 1, a, m, hh, rws, lane * 16, hsoff + (b0 + NB - 1) * 2048);
  WAITL0();
  SCHED();
  issue_w2_g5(L);
  STAMP(8);
  WAITV(6);
}
template <bool SQ>
DI void unit_q(const Ld& L, unsigned o0, unsigned b0, unsigned xa0, unsigned xa1, unsigned wl, unsigned par, unsigned lane, rsrc_t rws, unsigned qsoff, u64 (&sv)[16]) {
  f32x16 acc[4]; unsigned wa[4];
#pragma unroll
  for (int b = 0; b < 4; ++b) wa[b] = wl + ((o0 + (b0 + b) * 56) ^ par);
  kloop<4, true, SQ>(acc, wa, xa0, xa1, L, b0, lane);
  STAMP(1);
  const unsigned l32 = lane & 31, hh = lane >> 5;
#pragma unroll
  for (int b = 0; b < 4; ++b) {
    float c = colsum(b0 + b, l32);
    *(LAS float*)(L_CSF + (b0 + b) * 256 + lane * 4) = c;
  }
  float mu, var; rowstat((L.w & 3) * 32 + l32, mu, var);
  float a = __builtin_amdgcn_rsqf(var + 1e-5f), m = a * mu;
#pragma unroll
  for (int b = 0; b < 4; ++b) epi_tr<false>(acc[b], b0 + b, a, m, hh, rws, lane * 16, qsoff + (b0 + b) * 2048);
  STAMP(8);
  WAITV(0);
}
template <int NH, bool SQ>
DI void unit_kv(const Ld& L, unsigned o0, unsigned h0, unsigned xa0, unsigned xa1, unsigned wl, unsigned par, unsigned lane,
                bf16x8 (&w2r)[9][2], unsigned a20, unsigned a21, u64 (&sv)[16]) {
  f32x16 acc[2 * NH]; unsigned wa[2 * NH];
#pragma unroll
  for (int b = 0; b < 2 * NH; ++b) wa[b] = wl + ((o0 + (h0 + (b >> 1)) * 56 + (b & 1) * 256) ^ par);
  kloop<2 * NH, false, SQ>(acc, wa, xa0, xa1, L, 2 * h0, lane);
  STAMP(1);
  const unsigned l32 = lane & 31, hh = lane >> 5, g = L.w & 3;
  float mu, var; rowstat(g * 32 + l32, mu, var);
  float r = __builtin_amdgcn_rsqf(var + 1e-5f);
  *(LAS float*)(L_RS + hh * 512 + (g * 32 + l32) * 4) = hh ? r * mu : r;
  float cs[2 * NH];
#pragma unroll
  for (int b = 0; b < 2 * NH; ++b) cs[b] = colsum(2 * h0 + b, l32);
  WAITV(0);
  w2_to_regs<0, 6>(w2r, a20, a21);
  WAITL0();
  SCHED();
  issue_w2_h(L, 3, 0);
  issue_w2_g4(L);
  f32x4 ra[4], rm[4];
#pragma unroll
  for (int q = 0; q < 4; ++q) { ra[q] = ldf4(L_RS + (g * 32 + q * 8 + hh * 4) * 4); rm[q] = ldf4(L_RS + 512 + (g * 32 + q * 8 + hh * 4) * 4); }
  const float vpad = l32 == 28 ? 1.f : 0.f;
#pragma unroll
  for (int h = 0; h < NH; ++h) {
    float k[16], v[16];
#pragma unroll
    for (int i = 0; i < 16; ++i) {
      float kk = phi(fmaf(ra[i >> 2][i & 3], acc[2 * h][i], -rm[i >> 2][i & 3] * cs[2 * h]));
      float vv = fmaf(ra[i >> 2][i & 3], acc[2 * h + 1][i], -rm[i >> 2][i & 3] * cs[2 * h + 1]);
      k[i] = l32 < 28 ? kk : 0.f;
      v[i] = l32 < 28 ? vv : vpad;
    }
    f32x16 kv;
#pragma unroll
    for (int i = 0; i < 16; ++i) kv[i] = 0.f;
#pragma unroll
    for (int s = 0; s < 2; ++s) {
      u32x4 pk = {pk2(k[8 * s], k[8 * s + 1]), pk2(k[8 * s + 2], k[8 * s + 3]), pk2(k[8 * s + 4], k[8 * s + 5]), pk2(k[8 * s + 6], k[8 * s + 7])};
      u32x4 pv = {pk2(v[8 * s], v[8 * s + 1]), pk2(v[8 * s + 2], v[8 * s + 3]), pk2(v[8 * s + 4], v[8 * s + 5]), pk2(v[8 * s + 6], v[8 * s + 7])};
      kv = mfma(__builtin_bit_cast(bf16x8, pk), __builtin_bit_cast(bf16x8, pv), kv);
    }
#pragma unroll
    for (int q = 0; q < 4; ++q) {
      f32x4 t = {kv[4 * q], kv[4 * q + 1], kv[4 * q + 2], kv[4 * q + 3]};
      *(LAS f32x4*)(2 * STG + (((h0 + h) * 4 + g) * 4 + q) * 1024 + lane * 16) = t;
    }
  }
  WAITL0();
}

__global__ __launch_bounds__(512, 2) void fused_kernel(const char* __restrict__ x, const char* __restrict__ wqkv, const char* __restrict__ wo,
                                                       const char* __restrict__ w1, const char* __restrict__ b1, const char* __restrict__ w2,
                                                       const char* __restrict__ b2, char* __restrict__ out, char* ws) {
  extern __shared__ __attribute__((aligned(16))) char lds_dyn[];
  const unsigned tid = threadIdx.x, lane = tid & 63, w = __builtin_amdgcn_readfirstlane(tid >> 6);
  const unsigned xcd = blockIdx.x & 7, bi = blockIdx.x >> 3;
  u64 sv[16] = {0, 0, 0, 0, 0, 0, 0, 0, 0, 0, 0, 0, 0, 0, 0, 0};
  CYC(14);
  STAMP(0);
  const unsigned panel = (xcd & 3) * 4 + (bi >> 3), j = bi & 7;
  const bool is_w1 = xcd < 4, is_q = !is_w1 && j >= 6;
  const unsigned slot = is_w1 ? j : 8 + j;
  unsigned u2 = is_w1 ? xcd * 32 + bi : 128 + (xcd - 4) * 24 + (bi >> 3) * 6 + j;
  if (is_q) u2 = 0;
  const unsigned T = u2 / 7, ct = u2 - T * 7;
  unsigned seg0, seg1, o0;
  const unsigned kvh0 = j < 2 ? 3 * j : (j < 4 ? 2 * j + 2 : 3 * j - 2);
  if (is_w1) { seg0 = (448 * j) & ~127u; seg1 = seg0 + 256; o0 = (j & 1) * 64; }
  else if (is_q) { seg0 = (j - 6) * 384; seg1 = seg0 + 256; o0 = (j - 6) * 64; }
  else { seg0 = 896 + 128 * j; seg1 = 1792 + 128 * j; o0 = 56 * kvh0 - 128 * j; }

  Ld L;
  L.w = w;
  L.rs = is_w1 ? 3584 : 2688;
#define ROT 0
  L.c0 = ROT == 1 ? (panel * 2 + j) % 7 : (ROT == 2 ? (panel & 2) * 2 : (ROT == 3 ? ((panel & 3) * 7) / 4 : 0));
  L.rx = __builtin_amdgcn_make_buffer_rsrc((void*)(x + panel * 128 * 896), 0, 128 * 896, 0x00020000);
  L.rw = __builtin_amdgcn_make_buffer_rsrc((void*)(is_w1 ? w1 : wqkv), 0, 448 * L.rs, 0x00020000);
  L.r2 = __builtin_amdgcn_make_buffer_rsrc((void*)w2, 0, 1792 * 896, 0x00020000);
  L.ro = __builtin_amdgcn_make_buffer_rsrc((void*)wo, 0, 448 * 896, 0x00020000);
  const rsrc_t rws = __builtin_amdgcn_make_buffer_rsrc((void*)ws, 0, W_END, 0x00020000);
  {
    unsigned r8 = lane >> 3, c = ((lane & 7) ^ ((lane >> 2) & 4)) * 16;
    L.vx = (w * 16 + r8) * 896 + c;
    L.v2 = r8 * 896 + c + ct * 128;
    L.v2b = (r8 & 3) * 896 + c + ct * 128;
    unsigned rp = lane >> 5, sp = ((lane & 31) * 16) ^ (rp * 128);
    L.vw = (w * 8 + rp) * L.rs + ((sp & 256) ? seg1 : seg0) + (sp & 255);
  }
  const unsigned grp = w >> 2, b0w = grp * 4;
  u32x2 gen; unsigned bb[4];
  {
    unsigned zero = 0, bo = is_w1 ? (224 * j + b0w * 32 + (lane & 31)) * 2 : 0;
    unsigned bo3 = grp ? bo : bo + 192;
    asm volatile("s_nop 4\n\tglobal_load_dwordx2 %0, %5, %6 sc1\n\tglobal_load_ushort %1, %7, %8\n\tglobal_load_ushort %2, %7, %8 offset:64\n\t"
                 "global_load_ushort %3, %7, %8 offset:128\n\tglobal_load_ushort %4, %9, %8"
                 : "=&v"(gen), "=&v"(bb[0]), "=&v"(bb[1]), "=&v"(bb[2]), "=&v"(bb[3]) : "v"(zero), "s"(ws), "v"(bo), "s"(b1), "v"(bo3) : "memory");
  }
  issue_chunk(L, 0, 2 * STG);
  issue_chunk(L, 1, 0);
  const unsigned l32 = lane & 31, hh = lane >> 5, q4 = (lane & 15) >> 2, blk = (lane >> 4) & 1, p4 = lane & 3;
  unsigned xa0, xa1;
  {
    unsigned r = (w & 3) * 32 + l32, f = (r >> 1) & 1, base = XOFF + (r >> 3) * 1040 + (r & 7) * 128 + hh * 16;
    xa0 = base + f * 64; xa1 = base + (f ^ 1) * 64;
  }
  const unsigned par = (q4 & 1) * 128, wl = 1088 * (4 * hh + (q4 >> 1)) + (q4 & 1) * 512;
  o0 += blk * 32 + p4 * 8;
  const unsigned a20 = (4 * hh + q4) * 128 + (q4 >> 1) * 64 + blk * 32 + p4 * 8 + w * 6144, a21 = a20 ^ 64;
  asm volatile("s_waitcnt vmcnt(6)" : "+v"(gen), "+v"(bb[0]), "+v"(bb[1]), "+v"(bb[2]), "+v"(bb[3])::"memory");
  const u64 g0 = ((u64)(unsigned)__builtin_amdgcn_readfirstlane(gen[1]) << 32) | (unsigned)__builtin_amdgcn_readfirstlane(gen[0]);
  const u64 tag = g0 * 6364136223846793005ull + 1442695040888963407ull;
  BAR();
  STAMP(7);

  bf16x8 w2r[9][2];
  if (is_w1) {
    float bias[4];
#pragma unroll
    for (int b = 0; b < 4; ++b) bias[b] = __uint_as_float(bb[b] << 16);
    unsigned hsoff = W_H + ((panel * 4 + (w & 3)) * 112 + j * 14) * 1024;
    if (grp == 0) unit_w1<4, false>(L, o0, 0, bias, xa0, xa1, wl, par, lane, rws, hsoff, w2r, a20, a21, sv);
    else unit_w1<3, true>(L, o0, 4, bias, xa0, xa1, wl, par, lane, rws, hsoff, w2r, a20, a21, sv);
    BAR();
    if (tid == 0) __hip_atomic_store((GAS u64*)(ws + W_F1 + (panel * 16 + slot) * 128), tag, __ATOMIC_RELAXED, __HIP_MEMORY_SCOPE_AGENT);
  } else if (is_q) {
    unsigned qsoff = W_PQ + ((panel * 4 + (w & 3)) * 16 + (j - 6) * 8) * 2048;
    if (grp == 0) unit_q<false>(L, o0, 0, xa0, xa1, wl, par, lane, rws, qsoff, sv);
    else unit_q<true>(L, o0, 4, xa0, xa1, wl, par, lane, rws, qsoff, sv);
    BAR();
    if (tid == 0) __hip_atomic_store((GAS u64*)(ws + W_F1 + (panel * 16 + slot) * 128), tag, __ATOMIC_RELAXED, __HIP_MEMORY_SCOPE_AGENT);
    STAMP(2);
    const unsigned rj = panel * 2 + (j - 6), head = rj >> 1, sh = rj & 1;
    if (w == 0) {
      const unsigned gj = head < 6 ? head / 3 : (head < 10 ? (head - 2) >> 1 : (head + 2) / 3);
      wait_flag(ws, W_F1 + ((lane & 15) * 16 + 8 + gj) * 128, tag);
      STAMP(3);
      u32x4 t[16][2];
#pragma unroll
      for (int p = 0; p < 16; ++p) {
        t[p][0] = ldsc(rws, lane * 16, W_KVP + (p * 16 + head) * 4096 + sh * 2048);
        t[p][1] = ldsc(rws, lane * 16, W_KVP + (p * 16 + head) * 4096 + sh * 2048 + 1024);
      }
      f32x4 s0 = __builtin_bit_cast(f32x4, t[0][0]), s1 = __builtin_bit_cast(f32x4, t[0][1]);
#pragma unroll
      for (int p = 1; p < 16; ++p) { s0 += __builtin_bit_cast(f32x4, t[p][0]); s1 += __builtin_bit_cast(f32x4, t[p][1]); }
      u32x4 o = {pk2(s0[0], s0[1]), pk2(s0[2], s0[3]), pk2(s1[0], s1[1]), pk2(s1[2], s1[3])};
      stsc(o, rws, lane * 16, W_KVF + rj * 1024);
      WAITV(0);
      if (lane == 0) __hip_atomic_store((GAS u64*)(ws + W_FK + rj * 128), tag, __ATOMIC_RELAXED, __HIP_MEMORY_SCOPE_AGENT);
      STAMP(4);
    } else if (w == 1 && rj == 0) {
#pragma unroll 1
      for (int k = 0; k < 4; ++k) wait_flag(ws, W_F1 + (k * 64 + lane) * 128, tag);
      if (lane == 0) __hip_atomic_store((GAS u64*)(ws + W_GEN), tag, __ATOMIC_RELAXED, __HIP_MEMORY_SCOPE_AGENT);
    }
    STAMP(6); STAMP_OUT();
    return;
  } else {
    const bool three = j < 2 || j >= 4;
    if (grp == 0) {
      if (three) unit_kv<2, false>(L, o0, 0, xa0, xa1, wl, par, lane, w2r, a20, a21, sv);
      else unit_kv<1, false>(L, o0, 0, xa0, xa1, wl, par, lane, w2r, a20, a21, sv);
    } else unit_kv<1, true>(L, o0, three ? 2 : 1, xa0, xa1, wl, par, lane, w2r, a20, a21, sv);
    BAR();
    for (unsigned it = w; it < (three ? 12u : 8u); it += 8) {
      unsigned a = 2 * STG + ((it >> 2) * 16 + (it & 3)) * 1024 + lane * 16;
      f32x4 s = (ldf4(a) + ldf4(a + 4096)) + (ldf4(a + 8192) + ldf4(a + 12288));
      stsc(__builtin_bit_cast(u32x4, s), rws, lane * 16, W_KVP + (panel * 16 + kvh0) * 4096 + it * 1024);
    }
    WAITV(0);
    BAR();
    if (tid == 0) __hip_atomic_store((GAS u64*)(ws + W_F1 + (panel * 16 + slot) * 128), tag, __ATOMIC_RELAXED, __HIP_MEMORY_SCOPE_AGENT);
    issue_w2_h(L, 2, 2);
    WAITV(0);
    w2_to_regs<6, 9>(w2r, a20, a21);
    WAITL0();
    SCHED();
    issue_w2_g5(L);
  }
  STAMP(2);

  const unsigned p2 = T >> 1;
  const unsigned otok = T * 64 + (tid >> 3), ocol = ct * 64 + (tid & 7) * 8;
  const u32x4 xin = *(const GAS u32x4*)(x + (otok * 448 + ocol) * 2);
  const u32x4 bin = *(const GAS u32x4*)(b2 + ocol * 2);
  wait_flag(ws, W_F1 + (p2 * 16 + w) * 128, tag);
  STAMP(3);
  f32x16 acc[2][2];
#pragma unroll
  for (int i = 0; i < 4; ++i)
#pragma unroll
    for (int r = 0; r < 16; ++r) acc[i >> 1][i & 1][r] = 0.f;
  {
    const unsigned hb = W_H + (T * 2 * 112 + w * 14) * 1024;
    u32x4 h[14][2];
#pragma unroll
    for (int n = 0; n < 8; ++n) { h[n][0] = ldsc(rws, lane * 16, hb + n * 1024); h[n][1] = ldsc(rws, lane * 16, hb + (112 + n) * 1024); }
    SCHED();
#pragma unroll
    for (int n = 0; n < 14; ++n) {
      bf16x8 wa, wb;
      if (n < 9) { wa = w2r[n][0]; wb = w2r[n][1]; }
      else { wa = ldtr(a20 + ((n - 9) / 3) * STG + ((n - 9) % 3) * 2048, 1024); wb = ldtr(a21 + ((n - 9) / 3) * STG + ((n - 9) % 3) * 2048, 1024); }
      bf16x8 h0 = __builtin_bit_cast(bf16x8, h[n][0]), h1 = __builtin_bit_cast(bf16x8, h[n][1]);
      acc[0][0] = mfma(wa, h0, acc[0][0]);
      acc[0][1] = mfma(wb, h0, acc[0][1]);
      acc[1][0] = mfma(wa, h1, acc[1][0]);
      acc[1][1] = mfma(wb, h1, acc[1][1]);
      if (n + 8 < 14) { h[n + 8][0] = ldsc(rws, lane * 16, hb + (n + 8) * 1024); h[n + 8][1] = ldsc(rws, lane * 16, hb + (112 + n + 8) * 1024); }
      SCHED();
    }
  }
  STAMP(4);
  {
    unsigned fo = lane < 4 ? W_FK + (w * 4 + lane) * 128 : W_F1 + (p2 * 16 + 14 + (w >> 2)) * 128;
    wait_flag(ws, fo, tag);
    STAMP(5);
    u32x4 kf[2][2], pq[2][2][2];
#pragma unroll
    for (int hd = 0; hd < 2; ++hd)
#pragma unroll
      for (int s = 0; s < 2; ++s) {
        kf[hd][s] = ldsc(rws, lane * 16, W_KVF + ((w * 2 + hd) * 2 + s) * 1024);
        pq[hd][0][s] = ldsc(rws, lane * 16, W_PQ + (((T * 2) * 16 + w * 2 + hd) * 2 + s) * 1024);
        pq[hd][1][s] = ldsc(rws, lane * 16, W_PQ + (((T * 2 + 1) * 16 + w * 2 + hd) * 2 + s) * 1024);
      }
#pragma unroll
    for (int hd = 0; hd < 2; ++hd) {
      bf16x8 wf[2][2];
#pragma unroll
      for (int s = 0; s < 2; ++s) {
        const int n = 5 + hd * 2 + s;
        wf[s][0] = ldtr(a20 + (n / 3) * STG + (n % 3) * 2048, 1024);
        wf[s][1] = ldtr(a21 + (n / 3) * STG + (n % 3) * 2048, 1024);
      }
#pragma unroll
      for (int th = 0; th < 2; ++th) {
        f32x16 nm;
#pragma unroll
        for (int r = 0; r < 16; ++r) nm[r] = 0.f;
        nm = mfma(__builtin_bit_cast(bf16x8, kf[hd][0]), __builtin_bit_cast(bf16x8, pq[hd][th][0]), nm);
        nm = mfma(__builtin_bit_cast(bf16x8, kf[hd][1]), __builtin_bit_cast(bf16x8, pq[hd][th][1]), nm);
        float rd = __builtin_amdgcn_rcpf(bcast_hi(nm[12]) + 1e-6f);
        float o[16];
#pragma unroll
        for (int r = 0; r < 16; ++r) o[r] = (r >= 12 && hh) ? 0.f : nm[r] * rd;
#pragma unroll
        for (int s = 0; s < 2; ++s) {
          u32x4 po = {pk2(o[8 * s], o[8 * s + 1]), pk2(o[8 * s + 2], o[8 * s + 3]), pk2(o[8 * s + 4], o[8 * s + 5]), pk2(o[8 * s + 6], o[8 * s + 7])};
          acc[th][0] = mfma(wf[s][0], __builtin_bit_cast(bf16x8, po), acc[th][0]);
          acc[th][1] = mfma(wf[s][1], __builtin_bit_cast(bf16x8, po), acc[th][1]);
        }
      }
    }
  }
  STAMP(9);
  WAITL0();
  BAR();
  STAMP(10);
#pragma unroll
  for (int th = 0; th < 2; ++th)
#pragma unroll
    for (int cb = 0; cb < 2; ++cb)
#pragma unroll
      for (int q = 0; q < 4; ++q) {
        f32x4 t = {acc[th][cb][4 * q], acc[th][cb][4 * q + 1], acc[th][cb][4 * q + 2], acc[th][cb][4 * q + 3]};
        *(LAS f32x4*)(w * 17408 + (th * 32 + l32) * 272 + (cb * 32 + q * 8 + hh * 4) * 4) = t;
      }
  WAITL0();
  BAR();
  STAMP(11);
  {
    unsigned a = (tid >> 3) * 272 + (tid & 7) * 32;
    f32x4 s0 = ldf4(a), s1 = ldf4(a + 16);
#pragma unroll
    for (int p = 1; p < 8; ++p) { s0 += ldf4(a + p * 17408); s1 += ldf4(a + p * 17408 + 16); }
    float r[8];
#pragma unroll
    for (int i = 0; i < 4; ++i) {
      float lo = __uint_as_float(xin[i] << 16) + __uint_as_float(bin[i] << 16), hi = __uint_as_float(xin[i] & 0xffff0000u) + __uint_as_float(bin[i] & 0xffff0000u);
      r[2 * i] = lo + (i < 2 ? s0[2 * i] : s1[2 * i - 4]);
      r[2 * i + 1] = hi + (i < 2 ? s0[2 * i + 1] : s1[2 * i - 3]);
    }
    u32x4 o = {pk2(r[0], r[1]), pk2(r[2], r[3]), pk2(r[4], r[5]), pk2(r[6], r[7])};
    *(GAS u32x4*)(out + (otok * 448 + ocol) * 2) = o;
  }
  STAMP(6); STAMP_OUT();
}

__global__ void prk(char* ws) {
  __shared__ unsigned long long t0s[256];
  __shared__ float acc[3][16][2];
  unsigned b = threadIdx.x;
  const unsigned long long* s = (const unsigned long long*)(ws + W_DBG);
  unsigned long long m = ~0ull; for (int w = 0; w < 8; ++w) m = min(m, s[(b * 8 + w) * 16]);
  t0s[b] = m; __syncthreads();
  if (b == 0) {
    unsigned long long t0 = ~0ull; for (int i = 0; i < 256; ++i) t0 = min(t0, t0s[i]);
    int cnt[3] = {0,0,0};
    for (int r = 0; r < 3; ++r) for (int k = 0; k < 16; ++k) { acc[r][k][0] = 0; acc[r][k][1] = 0; }
    for (int i = 0; i < 256; ++i) { int r = ROLE(i); cnt[r]++; for (int k = 0; k < 14; ++k) { unsigned long long v = s[(i * 8) * 16 + k]; float d = v ? (v - t0) * 0.01f : 0.f; acc[r][k][0] += d; acc[r][k][1] = fmaxf(acc[r][k][1], d); } }
    float ck = (s[15] - s[14]) / ((s[6] - s[0]) * 10.0f);
    for (int r = 0; r < 3; ++r) {
      float (*a)[2] = acc[r]; float c = 1.f / cnt[r];
      printf("R%d ck%.2f s0 %.1f/%.1f s7 %.1f/%.1f s1 %.1f/%.1f s12 %.1f s13 %.1f s8 %.1f/%.1f s2 %.1f/%.1f s3 %.1f/%.1f s4 %.1f/%.1f s5 %.1f/%.1f s9 %.1f/%.1f s10 %.1f s11 %.1f s6 %.1f/%.1f\n", r, ck,
        a[0][0]*c, a[0][1], a[7][0]*c, a[7][1], a[1][0]*c, a[1][1], a[12][0]*c, a[13][0]*c, a[8][0]*c, a[8][1], a[2][0]*c, a[2][1], a[3][0]*c, a[3][1], a[4][0]*c, a[4][1], a[5][0]*c, a[5][1], a[9][0]*c, a[9][1], a[10][0]*c, a[11][0]*c, a[6][0]*c, a[6][1]);
    }
  }
}
extern "C" void kernel_launch(void* const* d_in, const int* in_sizes, int n_in, void* d_out, int out_size, void* d_ws, size_t ws_size,
                              hipStream_t stream) {
  (void)hipFuncSetAttribute((const void*)fused_kernel, hipFuncAttributeMaxDynamicSharedMemorySize, LDS_BYTES);
  fused_kernel<<<256, 512, LDS_BYTES, stream>>>((const char*)d_in[0], (const char*)d_in[1], (const char*)d_in[2], (const char*)d_in[4],
                                                (const char*)d_in[5], (const char*)d_in[6], (const char*)d_in[7], (char*)d_out, (char*)d_ws);
  prk<<<1,256,0,stream>>>((char*)d_ws);
}
```

```cpp
#define ROLE(b) (((b)&7)<4?0:((((b)>>3)&7)>=6?2:1))
#define STAMPS
#include <hip/hip_runtime.h>
#pragma clang diagnostic ignored "-Wint-to-pointer-cast"

typedef unsigned long long u64;
typedef __attribute__((ext_vector_type(8))) __bf16 bf16x8;
typedef __attribute__((ext_vector_type(2))) __bf16 bf16x2;
typedef __attribute__((ext_vector_type(4))) short s16x4;
typedef __attribute__((ext_vector_type(16))) float f32x16;
typedef __attribute__((ext_vector_type(4))) float f32x4;
typedef __attribute__((ext_vector_type(2))) float f32x2;
typedef __attribute__((ext_vector_type(4))) unsigned u32x4;
typedef __attribute__((ext_vector_type(2))) unsigned u32x2;
typedef __amdgpu_buffer_rsrc_t rsrc_t;
#define GAS __attribute__((address_space(1)))
#define LAS __attribute__((address_space(3)))
#define DI __device__ __forceinline__
#define SCHED() __builtin_amdgcn_sched_barrier(0)
#define BAR() __builtin_amdgcn_s_barrier()
#define WAITV(n) asm volatile("s_waitcnt vmcnt(" #n ")" ::: "memory")
#define WAITL0() asm volatile("s_waitcnt lgkmcnt(0)" ::: "memory")

constexpr unsigned STG = 51456;
constexpr unsigned XOFF = 34816;
constexpr unsigned L_CSP = 154368;
constexpr unsigned L_ST = 158464;
constexpr unsigned L_CSF = 159488;
constexpr unsigned L_RS = 161536;
constexpr unsigned LDS_BYTES = 163840;
constexpr unsigned W_GEN = 0;
constexpr unsigned W_F1 = 4096;
constexpr unsigned W_FK = W_F1 + 256 * 128;
constexpr unsigned W_KVF = 65536;
constexpr unsigned W_KVP = 131072;
constexpr unsigned W_PQ = W_KVP + (1u << 20);
constexpr unsigned W_H = W_PQ + (2u << 20);
constexpr unsigned W_END = W_H + 64u * 112u * 1024u;
constexpr unsigned W_DBG = 16u << 20;
constexpr int SPIN_MAX = 400000;

#define STAMP(i) do { SCHED(); sv[i] = __builtin_amdgcn_s_memrealtime(); SCHED(); } while (0)
#define CYC(i) do { sv[i] = __builtin_amdgcn_s_memtime(); } while (0)
#define STAMP_OUT() do { CYC(15); if ((threadIdx.x & 63) == 0) for (int i_ = 0; i_ < 16; ++i_) ((GAS u64*)(ws + W_DBG))[(blockIdx.x * 8 + (threadIdx.x >> 6)) * 16 + i_] = sv[i_]; } while (0)

DI void glds(unsigned ldsaddr, unsigned voff, rsrc_t r, unsigned soff) {
  asm volatile("s_mov_b32 m0, %0\n\ts_nop 0\n\tbuffer_load_dwordx4 %1, %2, %3 offen lds" ::"s"(ldsaddr), "v"(voff), "s"(r), "s"(soff) : "memory");
}
DI bf16x8 ld128(unsigned a) { return *(LAS bf16x8*)a; }
DI f32x4 ldf4(unsigned a) { return *(LAS f32x4*)a; }
DI bf16x8 ldtr(unsigned a, unsigned hi) {
  s16x4 lo = __builtin_amdgcn_ds_read_tr16_b64_v4i16((LAS s16x4*)a);
  s16x4 h = __builtin_amdgcn_ds_read_tr16_b64_v4i16((LAS s16x4*)(a + hi));
  return __builtin_bit_cast(bf16x8, __builtin_shufflevector(lo, h, 0, 1, 2, 3, 4, 5, 6, 7));
}
DI f32x16 mfma(bf16x8 a, bf16x8 b, f32x16 c) { return __builtin_amdgcn_mfma_f32_32x32x16_bf16(a, b, c, 0, 0, 0); }
DI float dot2(unsigned a, unsigned b, float c) { return __builtin_amdgcn_fdot2_f32_bf16(__builtin_bit_cast(bf16x2, a), __builtin_bit_cast(bf16x2, b), c, false); }
DI unsigned pk2(float a, float b) { f32x2 v = {a, b}; return __builtin_bit_cast(unsigned, __builtin_convertvector(v, bf16x2)); }
DI float halfsum(float x) { auto t = __builtin_amdgcn_permlane32_swap(__float_as_uint(x), __float_as_uint(x), false, false); return __uint_as_float(t[0]) + __uint_as_float(t[1]); }
DI float bcast_hi(float x) { auto t = __builtin_amdgcn_permlane32_swap(__float_as_uint(x), __float_as_uint(x), false, false); return __uint_as_float(t[1]); }
DI u32x4 ldsc(rsrc_t r, unsigned voff, unsigned soff) { return __builtin_amdgcn_raw_buffer_load_b128(r, voff, soff, 16); }
DI void stsc(u32x4 v, rsrc_t r, unsigned voff, unsigned soff) { __builtin_amdgcn_raw_buffer_store_b128(v, r, voff, soff, 16); }
DI float phi(float t) { return (t > 0.f ? t + 1.f : __builtin_amdgcn_exp2f(t * 1.4426950408889634f)) + 1e-6f; }
DI void wait_flag(const char* ws, unsigned off, u64 tag) {
  for (int s = 0; s < SPIN_MAX; ++s) {
    u64 v = __hip_atomic_load((GAS u64*)(ws + off), __ATOMIC_RELAXED, __HIP_MEMORY_SCOPE_AGENT);
    if (__all(v == tag)) break;
    __builtin_amdgcn_s_sleep(1);
  }
  __builtin_amdgcn_fence(__ATOMIC_ACQUIRE, "wavefront");
}

struct Ld {
  rsrc_t rx, rw, r2, ro;
  unsigned vx, vw, v2, v2b;
  unsigned rs;
  unsigned c0;
  unsigned w;
};
DI void issue_chunk(const Ld& L, unsigned i, unsigned sb) {
  unsigned c = L.c0 + i; c = c >= 7 ? c - 7 : c;
  unsigned sx = c * 128, sw = c * 64 * L.rs;
  unsigned lx = sb + XOFF + L.w * 2080, lw = sb + L.w * 4352;
  glds(lx, L.vx, L.rx, sx);
  glds(lw, L.vw, L.rw, sw);
  glds(lw + 1088, L.vw, L.rw, sw + 2 * L.rs);
  glds(lx + 1040, L.vx, L.rx, sx + 7168);
  glds(lw + 2176, L.vw, L.rw, sw + 4 * L.rs);
  glds(lw + 3264, L.vw, L.rw, sw + 6 * L.rs);
}
DI void issue_w2_h(const Ld& L, unsigned gi, unsigned reg) {
  unsigned l = reg * STG + L.w * 6144, s = (L.w * 224 + gi * 48) * 896;
#pragma unroll
  for (int j = 0; j < 6; ++j) glds(l + j * 1024, L.v2, L.r2, s + j * 7168);
}
DI void issue_w2_g4(const Ld& L) {
  unsigned l = STG + L.w * 6144, s = (L.w * 224 + 192) * 896, so = L.w * 56 * 896;
#pragma unroll
  for (int j = 0; j < 4; ++j) glds(l + j * 1024, L.v2, L.r2, s + j * 7168);
  glds(l + 4096, L.v2, L.ro, so);
  glds(l + 5120, L.v2, L.ro, so + 7168);
}
DI void issue_w2_g5(const Ld& L) {
  unsigned l = 2 * STG + L.w * 6144, so = L.w * 56 * 896;
  glds(l, L.v2, L.ro, so + 2 * 7168);
  glds(l + 1024, L.v2b, L.ro, so + 3 * 7168);
  so += 28 * 896;
  glds(l + 2048, L.v2, L.ro, so);
  glds(l + 3072, L.v2, L.ro, so + 7168);
  glds(l + 4096, L.v2, L.ro, so + 2 * 7168);
  glds(l + 5120, L.v2b, L.ro, so + 3 * 7168);
}

template <int NB> struct Frag { bf16x8 x; bf16x8 w[NB]; };
template <int NB, int S> DI void ld_frag(Frag<NB>& f, unsigned sb, const unsigned (&wa)[NB], unsigned xa0, unsigned xa1) {
  f.x = ld128(sb + (S < 2 ? xa0 : xa1) + (S & 1) * 32);
#pragma unroll
  for (int b = 0; b < NB; ++b) f.w[b] = ldtr(sb + wa[b] + S * 8704, 2176);
}
struct G6 { rsrc_t ra, rb; unsigned va, vb, la, lb, sa, sb, dla, dlb, dsb; };
template <int K> DI void g6_issue(const G6& g) {
  if (K == 0) glds(g.la, g.va, g.ra, g.sa);
  else if (K == 3) glds(g.la + g.dla, g.va, g.ra, g.sa + 7168);
  else { constexpr int j = K < 3 ? K - 1 : K - 2; glds(g.lb + j * g.dlb, g.vb, g.rb, g.sb + j * g.dsb); }
}
template <int NB, bool TR, bool SQ, int SN, int G>
DI void step(const Frag<NB>& f, Frag<NB>& n, unsigned sn, const unsigned (&wa)[NB], unsigned xa0, unsigned xa1, const G6& g6,
             f32x16 (&acc)[NB], float (&cs)[NB], float (&st)[2], bool docs) {
  const u32x4 xv = __builtin_bit_cast(u32x4, f.x);
#pragma unroll
  for (int b = 0; b < NB; ++b) {
    acc[b] = TR ? mfma(f.w[b], f.x, acc[b]) : mfma(f.x, f.w[b], acc[b]);
    SCHED();
    if (SN >= 0) {
      if (b == 0) n.x = ld128(sn + (SN < 2 ? xa0 : xa1) + (SN & 1) * 32);
      if (2 * b < NB) n.w[2 * b] = ldtr(sn + wa[2 * b] + SN * 8704, 2176);
      if (2 * b + 1 < NB) n.w[2 * b + 1] = ldtr(sn + wa[2 * b + 1] + SN * 8704, 2176);
    }
    if (b >= NB - 2) {
      constexpr int NB2 = NB - 2;
      st[0] = dot2(xv[2 * (b - NB2)], SQ ? xv[2 * (b - NB2)] : 0x3f803f80u, st[0]);
      st[1] = dot2(xv[2 * (b - NB2) + 1], SQ ? xv[2 * (b - NB2) + 1] : 0x3f803f80u, st[1]);
      asm volatile("" : "+v"(st[0]), "+v"(st[1]));
    }
    if (G >= 0 && b == NB - 2) g6_issue<G < 0 ? 0 : G>(g6);
    if (G >= 0 && b == NB - 1) g6_issue<G < 0 ? 0 : G + 1>(g6);
    SCHED();
  }
  if (docs) {
#pragma unroll
    for (int j = 0; j < 4; ++j)
#pragma unroll
      for (int b = 0; b < NB; ++b) cs[b] = dot2(__builtin_bit_cast(u32x4, f.w[b])[j], 0x3f803f80u, cs[b]);
  }
  SCHED();
}
template <int NB, bool TR, bool SQ>
DI void kloop(f32x16 (&acc)[NB], const unsigned (&wa)[NB], unsigned xa0, unsigned xa1, const Ld& L, unsigned b0, unsigned lane) {
  float cs[NB]; float st[2] = {0.f, 0.f};
  const unsigned g = L.w & 3;
#pragma unroll
  for (int b = 0; b < NB; ++b) { cs[b] = 0.f; for (int r = 0; r < 16; ++r) acc[b][r] = 0.f; }
  Frag<NB> f0, f1;
  unsigned sb = 2 * STG, fb = STG;
  ld_frag<NB, 0>(f0, sb, wa, xa0, xa1);
#pragma unroll 1
  for (unsigned i = 0; i < 7; ++i) {
    G6 g6;
    {
      const bool k = i < 5;
      unsigned c = L.c0 + i + 2; c = c >= 7 ? c - 7 : c;
      const unsigned s2 = (L.w * 224 + (i - 5) * 48) * 896, l2 = fb + L.w * 6144;
      g6.ra = k ? L.rx : L.r2; g6.rb = k ? L.rw : L.r2;
      g6.va = k ? L.vx : L.v2; g6.vb = k ? L.vw : L.v2;
      g6.la = k ? fb + XOFF + L.w * 2080 : l2; g6.lb = k ? fb + L.w * 4352 : l2 + 2048;
      g6.sa = k ? c * 128 : s2; g6.sb = k ? c * 64 * L.rs : s2 + 2 * 7168;
      g6.dla = k ? 1040 : 1024; g6.dlb = k ? 1088 : 1024; g6.dsb = k ? 2 * L.rs : 7168;
    }
    const unsigned nb = sb == 2 * STG ? 0 : sb + STG;
    SCHED();
    step<NB, TR, SQ, 1, 0>(f0, f1, sb, wa, xa0, xa1, g6, acc, cs, st, g == 0);
    step<NB, TR, SQ, 2, 2>(f1, f0, sb, wa, xa0, xa1, g6, acc, cs, st, g == 1);
    step<NB, TR, SQ, 3, 4>(f0, f1, sb, wa, xa0, xa1, g6, acc, cs, st, g == 2);
    __builtin_amdgcn_s_waitcnt(0xC07F);
    WAITV(6);
    BAR();
    SCHED();
    step<NB, TR, SQ, 0, -1>(f1, f0, nb, wa, xa0, xa1, g6, acc, cs, st, g == 3);
    fb = sb; sb = nb;
  }
  SCHED();
  *(LAS float*)(L_ST + (SQ ? 512 : 0) + (g * 32 + (lane & 31)) * 4) = halfsum(st[0] + st[1]);
#pragma unroll
  for (int b = 0; b < NB; ++b) *(LAS float*)(L_CSP + (((b0 + b) * 4 + g) * 32 + (lane & 31)) * 4) = halfsum(cs[b]);
  WAITL0();
  BAR();
}
DI float colsum(unsigned blk, unsigned l32) {
  unsigned a = L_CSP + (blk * 128 + l32) * 4;
  return (*(LAS float*)a + *(LAS float*)(a + 128)) + (*(LAS float*)(a + 256) + *(LAS float*)(a + 384));
}
DI void rowstat(unsigned tok, float& mu, float& var) {
  float s1 = *(LAS float*)(L_ST + tok * 4), s2 = *(LAS float*)(L_ST + 512 + tok * 4);
  mu = s1 * (1.f / 448.f);
  var = fmaxf(s2 * (1.f / 448.f) - mu * mu, 0.f);
}
template <int N0, int N1> DI void w2_to_regs(bf16x8 (&w2r)[9][2], unsigned a20, unsigned a21) {
#pragma unroll
  for (int n = N0; n < N1; ++n) {
    w2r[n][0] = ldtr(a20 + (n / 3) * STG + (n % 3) * 2048, 1024);
    w2r[n][1] = ldtr(a21 + (n / 3) * STG + (n % 3) * 2048, 1024);
  }
}

template <bool GELU> DI void epi_tr(const f32x16& acc, unsigned blk, float a, float m, unsigned hh, rsrc_t rws, unsigned voff, unsigned soff) {
  float v[16];
#pragma unroll
  for (int q = 0; q < 4; ++q) {
    f32x4 c = ldf4(L_CSF + blk * 256 + q * 32 + hh * 16);
    f32x4 b = {0.f, 0.f, 0.f, 0.f};
    if (GELU) b = ldf4(L_CSF + blk * 256 + 128 + q * 32 + hh * 16);
#pragma unroll
    for (int j = 0; j < 4; ++j) {
      float u = fmaf(a, acc[q * 4 + j], fmaf(-m, c[j], b[j]));
      if (GELU) {
        float p = fmaf(u * u, -0.10294323970248637f, -2.302208198144325f);
        float e = __builtin_amdgcn_exp2f(u * p);
        v[q * 4 + j] = u * __builtin_amdgcn_rcpf(1.f + e);
      } else {
        float t = phi(u);
        v[q * 4 + j] = (q == 3 && hh) ? 0.f : t;
      }
    }
  }
  u32x4 p0 = {pk2(v[0], v[1]), pk2(v[2], v[3]), pk2(v[4], v[5]), pk2(v[6], v[7])};
  u32x4 p1 = {pk2(v[8], v[9]), pk2(v[10], v[11]), pk2(v[12], v[13]), pk2(v[14], v[15])};
  stsc(p0, rws, voff, soff);
  stsc(p1, rws, voff, soff + 1024);
}

template <int NB, bool SQ>
DI void unit_w1(const Ld& L, unsigned o0, unsigned b0, const float (&bias)[4], unsigned xa0, unsigned xa1, unsigned wl, unsigned par, unsigned lane,
                rsrc_t rws, unsigned hsoff, bf16x8 (&w2r)[9][2], unsigned a20, unsigned a21, u64 (&sv)[16]) {
  f32x16 acc[NB]; unsigned wa[NB];
#pragma unroll
  for (int b = 0; b < NB; ++b) wa[b] = wl + ((o0 + (b0 + b) * 64) ^ par);
  kloop<NB, true, SQ>(acc, wa, xa0, xa1, L, b0, lane);
  STAMP(1);
  issue_w2_h(L, 2, 2);
  const unsigned l32 = lane & 31, hh = lane >> 5;
#pragma unroll
  for (int b = 0; b < NB; ++b) {
    float c = colsum(b0 + b, l32);
    *(LAS float*)(L_CSF + (b0 + b) * 256 + lane * 4) = hh ? bias[b] : c;
  }
  float mu, var; rowstat((L.w & 3) * 32 + l32, mu, var);
  float r = __builtin_amdgcn_rsqf(var + 1e-5f);
  float a = r * __builtin_amdgcn_rsqf(var * r * r + 1e-5f), m = a * mu;
  WAITV(6);
  w2_to_regs<0, 6>(w2r, a20, a21);
  WAITL0();
  SCHED();
  STAMP(12);
  issue_w2_h(L, 3, 0);
  issue_w2_g4(L);
  STAMP(13);
#pragma unroll
  for (int b = 0; b < NB - 1; ++b) { epi_tr<true>(acc[b], b0 + b, a, m, hh, rws, lane * 16, hsoff + (b0 + b) * 2048); SCHED(); }
  if (NB == 4) WAITV(18); else WAITV(16);
  w2_to_regs<6, 9>(w2r, a20, a21);
  epi_tr<true>(acc[NB - 1], b0 + NB - 1, a, m, hh, rws, lane * 16, hsoff + (b0 + NB - 1) * 2048);
  WAITL0();
  SCHED();
  issue_w2_g5(L);
  STAMP(8);
  WAITV(6);
}
template <bool SQ>
DI void unit_q(const Ld& L, unsigned o0, unsigned b0, unsigned xa0, unsigned xa1, unsigned wl, unsigned par, unsigned lane, rsrc_t rws, unsigned qsoff, u64 (&sv)[16]) {
  f32x16 acc[4]; unsigned wa[4];
#pragma unroll
  for (int b = 0; b < 4; ++b) wa[b] = wl + ((o0 + (b0 + b) * 56) ^ par);
  kloop<4, true, SQ>(acc, wa, xa0, xa1, L, b0, lane);
  STAMP(1);
  const unsigned l32 = lane & 31, hh = lane >> 5;
#pragma unroll
  for (int b = 0; b < 4; ++b) {
    float c = colsum(b0 + b, l32);
    *(LAS float*)(L_CSF + (b0 + b) * 256 + lane * 4) = c;
  }
  float mu, var; rowstat((L.w & 3) * 32 + l32, mu, var);
  float a = __builtin_amdgcn_rsqf(var + 1e-5f), m = a * mu;
#pragma unroll
  for (int b = 0; b < 4; ++b) epi_tr<false>(acc[b], b0 + b, a, m, hh, rws, lane * 16, qsoff + (b0 + b) * 2048);
  STAMP(8);
  WAITV(0);
}
template <int NH, bool SQ>
DI void unit_kv(const Ld& L, unsigned o0, unsigned h0, unsigned xa0, unsigned xa1, unsigned wl, unsigned par, unsigned lane,
                bf16x8 (&w2r)[9][2], unsigned a20, unsigned a21, u64 (&sv)[16]) {
  f32x16 acc[2 * NH]; unsigned wa[2 * NH];
#pragma unroll
  for (int b = 0; b < 2 * NH; ++b) wa[b] = wl + ((o0 + (h0 + (b >> 1)) * 56 + (b & 1) * 256) ^ par);
  kloop<2 * NH, false, SQ>(acc, wa, xa0, xa1, L, 2 * h0, lane);
  STAMP(1);
  const unsigned l32 = lane & 31, hh = lane >> 5, g = L.w & 3;
  float mu, var; rowstat(g * 32 + l32, mu, var);
  float r = __builtin_amdgcn_rsqf(var + 1e-5f);
  *(LAS float*)(L_RS + hh * 512 + (g * 32 + l32) * 4) = hh ? r * mu : r;
  float cs[2 * NH];
#pragma unroll
  for (int b = 0; b < 2 * NH; ++b) cs[b] = colsum(2 * h0 + b, l32);
  WAITV(0);
  w2_to_regs<0, 6>(w2r, a20, a21);
  WAITL0();
  SCHED();
  issue_w2_h(L, 3, 0);
  issue_w2_g4(L);
  f32x4 ra[4], rm[4];
#pragma unroll
  for (int q = 0; q < 4; ++q) { ra[q] = ldf4(L_RS + (g * 32 + q * 8 + hh * 4) * 4); rm[q] = ldf4(L_RS + 512 + (g * 32 + q * 8 + hh * 4) * 4); }
  const float vpad = l32 == 28 ? 1.f : 0.f;
#pragma unroll
  for (int h = 0; h < NH; ++h) {
    float k[16], v[16];
#pragma unroll
    for (int i = 0; i < 16; ++i) {
      float kk = phi(fmaf(ra[i >> 2][i & 3], acc[2 * h][i], -rm[i >> 2][i & 3] * cs[2 * h]));
      float vv = fmaf(ra[i >> 2][i & 3], acc[2 * h + 1][i], -rm[i >> 2][i & 3] * cs[2 * h + 1]);
      k[i] = l32 < 28 ? kk : 0.f;
      v[i] = l32 < 28 ? vv : vpad;
    }
    f32x16 kv;
#pragma unroll
    for (int i = 0; i < 16; ++i) kv[i] = 0.f;
#pragma unroll
    for (int s = 0; s < 2; ++s) {
      u32x4 pk = {pk2(k[8 * s], k[8 * s + 1]), pk2(k[8 * s + 2], k[8 * s + 3]), pk2(k[8 * s + 4], k[8 * s + 5]), pk2(k[8 * s + 6], k[8 * s + 7])};
      u32x4 pv = {pk2(v[8 * s], v[8 * s + 1]), pk2(v[8 * s + 2], v[8 * s + 3]), pk2(v[8 * s + 4], v[8 * s + 5]), pk2(v[8 * s + 6], v[8 * s + 7])};
      kv = mfma(__builtin_bit_cast(bf16x8, pk), __builtin_bit_cast(bf16x8, pv), kv);
    }
#pragma unroll
    for (int q = 0; q < 4; ++q) {
      f32x4 t = {kv[4 * q], kv[4 * q + 1], kv[4 * q + 2], kv[4 * q + 3]};
      *(LAS f32x4*)(2 * STG + (((h0 + h) * 4 + g) * 4 + q) * 1024 + lane * 16) = t;
    }
  }
  WAITL0();
}

__global__ __launch_bounds__(512, 2) void fused_kernel(const char* __restrict__ x, const char* __restrict__ wqkv, const char* __restrict__ wo,
                                                       const char* __restrict__ w1, const char* __restrict__ b1, const char* __restrict__ w2,
                                                       const char* __restrict__ b2, char* __restrict__ out, char* ws) {
  extern __shared__ __attribute__((aligned(16))) char lds_dyn[];
  const unsigned tid = threadIdx.x, lane = tid & 63, w = __builtin_amdgcn_readfirstlane(tid >> 6);
  const unsigned xcd = blockIdx.x & 7, bi = blockIdx.x >> 3;
  u64 sv[16] = {0, 0, 0, 0, 0, 0, 0, 0, 0, 0, 0, 0, 0, 0, 0, 0};
  CYC(14);
  STAMP(0);
  const unsigned panel = (xcd & 3) * 4 + (bi >> 3), j = bi & 7;
  const bool is_w1 = xcd < 4, is_q = !is_w1 && j >= 6;
  const unsigned slot = is_w1 ? j : 8 + j;
  unsigned u2 = is_w1 ? xcd * 32 + bi : 128 + (xcd - 4) * 24 + (bi >> 3) * 6 + j;
  if (is_q) u2 = 0;
  const unsigned T = u2 / 7, ct = u2 - T * 7;
  unsigned seg0, seg1, o0;
  const unsigned kvh0 = j < 2 ? 3 * j : (j < 4 ? 2 * j + 2 : 3 * j - 2);
  if (is_w1) { seg0 = (448 * j) & ~127u; seg1 = seg0 + 256; o0 = (j & 1) * 64; }
  else if (is_q) { seg0 = (j - 6) * 384; seg1 = seg0 + 256; o0 = (j - 6) * 64; }
  else { seg0 = 896 + 128 * j; seg1 = 1792 + 128 * j; o0 = 56 * kvh0 - 128 * j; }

  Ld L;
  L.w = w;
  L.rs = is_w1 ? 3584 : 2688;
#define ROT 0
  L.c0 = ROT == 1 ? (panel * 2 + j) % 7 : (ROT == 2 ? (panel & 2) * 2 : (ROT == 3 ? ((panel & 3) * 7) / 4 : 0));
  L.rx = __builtin_amdgcn_make_buffer_rsrc((void*)(x + panel * 128 * 896), 0, 128 * 896, 0x00020000);
  L.rw = __builtin_amdgcn_make_buffer_rsrc((void*)(is_w1 ? w1 : wqkv), 0, 448 * L.rs, 0x00020000);
  L.r2 = __builtin_amdgcn_make_buffer_rsrc((void*)w2, 0, 1792 * 896, 0x00020000);
  L.ro = __builtin_amdgcn_make_buffer_rsrc((void*)wo, 0, 448 * 896, 0x00020000);
  const rsrc_t rws = __builtin_amdgcn_make_buffer_rsrc((void*)ws, 0, W_END, 0x00020000);
  {
    unsigned r8 = lane >> 3, c = ((lane & 7) ^ ((lane >> 2) & 4)) * 16;
    L.vx = (w * 16 + r8) * 896 + c;
    L.v2 = r8 * 896 + c + ct * 128;
    L.v2b = (r8 & 3) * 896 + c + ct * 128;
    unsigned rp = lane >> 5, sp = ((lane & 31) * 16) ^ (rp * 128);
    L.vw = (w * 8 + rp) * L.rs + ((sp & 256) ? seg1 : seg0) + (sp & 255);
  }
  const unsigned grp = w >> 2, b0w = grp * 4;
  u32x2 gen; unsigned bb[4];
  {
    unsigned zero = 0, bo = is_w1 ? (224 * j + b0w * 32 + (lane & 31)) * 2 : 0;
    unsigned bo3 = grp ? bo : bo + 192;
    asm volatile("s_nop 4\n\tglobal_load_dwordx2 %0, %5, %6 sc1\n\tglobal_load_ushort %1, %7, %8\n\tglobal_load_ushort %2, %7, %8 offset:64\n\t"
                 "global_load_ushort %3, %7, %8 offset:128\n\tglobal_load_ushort %4, %9, %8"
                 : "=&v"(gen), "=&v"(bb[0]), "=&v"(bb[1]), "=&v"(bb[2]), "=&v"(bb[3]) : "v"(zero), "s"(ws), "v"(bo), "s"(b1), "v"(bo3) : "memory");
  }
  issue_chunk(L, 0, 2 * STG);
  issue_chunk(L, 1, 0);
  const unsigned l32 = lane & 31, hh = lane >> 5, q4 = (lane & 15) >> 2, blk = (lane >> 4) & 1, p4 = lane & 3;
  unsigned xa0, xa1;
  {
    unsigned r = (w & 3) * 32 + l32, f = (r >> 1) & 1, base = XOFF + (r >> 3) * 1040 + (r & 7) * 128 + hh * 16;
    xa0 = base + f * 64; xa1 = base + (f ^ 1) * 64;
  }
  const unsigned par = (q4 & 1) * 128, wl = 1088 * (4 * hh + (q4 >> 1)) + (q4 & 1) * 512;
  o0 += blk * 32 + p4 * 8;
  const unsigned a20 = (4 * hh + q4) * 128 + (q4 >> 1) * 64 + blk * 32 + p4 * 8 + w * 6144, a21 = a20 ^ 64;
  asm volatile("s_waitcnt vmcnt(6)" : "+v"(gen), "+v"(bb[0]), "+v"(bb[1]), "+v"(bb[2]), "+v"(bb[3])::"memory");
  const u64 g0 = ((u64)(unsigned)__builtin_amdgcn_readfirstlane(gen[1]) << 32) | (unsigned)__builtin_amdgcn_readfirstlane(gen[0]);
  const u64 tag = g0 * 6364136223846793005ull + 1442695040888963407ull;
  BAR();
  STAMP(7);

  bf16x8 w2r[9][2];
  if (is_w1) {
    float bias[4];
#pragma unroll
    for (int b = 0; b < 4; ++b) bias[b] = __uint_as_float(bb[b] << 16);
    unsigned hsoff = W_H + ((panel * 4 + (w & 3)) * 112 + j * 14) * 1024;
    if (grp == 0) unit_w1<4, false>(L, o0, 0, bias, xa0, xa1, wl, par, lane, rws, hsoff, w2r, a20, a21, sv);
    else unit_w1<3, true>(L, o0, 4, bias, xa0, xa1, wl, par, lane, rws, hsoff, w2r, a20, a21, sv);
    BAR();
    if (tid == 0) __hip_atomic_store((GAS u64*)(ws + W_F1 + (panel * 16 + slot) * 128), tag, __ATOMIC_RELAXED, __HIP_MEMORY_SCOPE_AGENT);
  } else if (is_q) {
    unsigned qsoff = W_PQ + ((panel * 4 + (w & 3)) * 16 + (j - 6) * 8) * 2048;
    if (grp == 0) unit_q<false>(L, o0, 0, xa0, xa1, wl, par, lane, rws, qsoff, sv);
    else unit_q<true>(L, o0, 4, xa0, xa1, wl, par, lane, rws, qsoff, sv);
    BAR();
    if (tid == 0) __hip_atomic_store((GAS u64*)(ws + W_F1 + (panel * 16 + slot) * 128), tag, __ATOMIC_RELAXED, __HIP_MEMORY_SCOPE_AGENT);
    STAMP(2);
    const unsigned rj = panel * 2 + (j - 6), head = rj >> 1, sh = rj & 1;
    if (w == 0) {
      const unsigned gj = head < 6 ? head / 3 : (head < 10 ? (head - 2) >> 1 : (head + 2) / 3);
      wait_flag(ws, W_F1 + ((lane & 15) * 16 + 8 + gj) * 128, tag);
      STAMP(3);
      u32x4 t[16][2];
#pragma unroll
      for (int p = 0; p < 16; ++p) {
        t[p][0] = ldsc(rws, lane * 16, W_KVP + (p * 16 + head) * 4096 + sh * 2048);
        t[p][1] = ldsc(rws, lane * 16, W_KVP + (p * 16 + head) * 4096 + sh * 2048 + 1024);
      }
      f32x4 s0 = __builtin_bit_cast(f32x4, t[0][0]), s1 = __builtin_bit_cast(f32x4, t[0][1]);
#pragma unroll
      for (int p = 1; p < 16; ++p) { s0 += __builtin_bit_cast(f32x4, t[p][0]); s1 += __builtin_bit_cast(f32x4, t[p][1]); }
      u32x4 o = {pk2(s0[0], s0[1]), pk2(s0[2], s0[3]), pk2(s1[0], s1[1]), pk2(s1[2], s1[3])};
      stsc(o, rws, lane * 16, W_KVF + rj * 1024);
      WAITV(0);
      if (lane == 0) __hip_atomic_store((GAS u64*)(ws + W_FK + rj * 128), tag, __ATOMIC_RELAXED, __HIP_MEMORY_SCOPE_AGENT);
      STAMP(4);
    } else if (w == 1 && rj == 0) {
#pragma unroll 1
      for (int k = 0; k < 4; ++k) wait_flag(ws, W_F1 + (k * 64 + lane) * 128, tag);
      if (lane == 0) __hip_atomic_store((GAS u64*)(ws + W_GEN), tag, __ATOMIC_RELAXED, __HIP_MEMORY_SCOPE_AGENT);
    }
    STAMP(6); STAMP_OUT();
    return;
  } else {
    const bool three = j < 2 || j >= 4;
    if (grp == 0) {
      if (three) unit_kv<2, false>(L, o0, 0, xa0, xa1, wl, par, lane, w2r, a20, a21, sv);
      else unit_kv<1, false>(L, o0, 0, xa0, xa1, wl, par, lane, w2r, a20, a21, sv);
    } else unit_kv<1, true>(L, o0, three ? 2 : 1, xa0, xa1, wl, par, lane, w2r, a20, a21, sv);
    BAR();
    for (unsigned it = w; it < (three ? 12u : 8u); it += 8) {
      unsigned a = 2 * STG + ((it >> 2) * 16 + (it & 3)) * 1024 + lane * 16;
      f32x4 s = (ldf4(a) + ldf4(a + 4096)) + (ldf4(a + 8192) + ldf4(a + 12288));
      stsc(__builtin_bit_cast(u32x4, s), rws, lane * 16, W_KVP + (panel * 16 + kvh0) * 4096 + it * 1024);
    }
    WAITV(0);
    BAR();
    if (tid == 0) __hip_atomic_store((GAS u64*)(ws + W_F1 + (panel * 16 + slot) * 128), tag, __ATOMIC_RELAXED, __HIP_MEMORY_SCOPE_AGENT);
    issue_w2_h(L, 2, 2);
    WAITV(0);
    w2_to_regs<6, 9>(w2r, a20, a21);
    WAITL0();
    SCHED();
    issue_w2_g5(L);
  }
  STAMP(2);

  const unsigned p2 = T >> 1;
  const unsigned otok = T * 64 + (tid >> 3), ocol = ct * 64 + (tid & 7) * 8;
  const u32x4 xin = *(const GAS u32x4*)(x + (otok * 448 + ocol) * 2);
  const u32x4 bin = *(const GAS u32x4*)(b2 + ocol * 2);
  wait_flag(ws, W_F1 + (p2 * 16 + w) * 128, tag);
  STAMP(3);
  f32x16 acc[2][2];
#pragma unroll
  for (int i = 0; i < 4; ++i)
#pragma unroll
    for (int r = 0; r < 16; ++r) acc[i >> 1][i & 1][r] = 0.f;
  {
    const unsigned hb = W_H + (T * 2 * 112 + w * 14) * 1024;
    u32x4 h[14][2];
#pragma unroll
    for (int n = 0; n < 8; ++n) { h[n][0] = ldsc(rws, lane * 16, hb + n * 1024); h[n][1] = ldsc(rws, lane * 16, hb + (112 + n) * 1024); }
    SCHED();
#pragma unroll
    for (int n = 0; n < 14; ++n) {
      bf16x8 wa, wb;
      if (n < 9) { wa = w2r[n][0]; wb = w2r[n][1]; }
      else { wa = ldtr(a20 + ((n - 9) / 3) * STG + ((n - 9) % 3) * 2048, 1024); wb = ldtr(a21 + ((n - 9) / 3) * STG + ((n - 9) % 3) * 2048, 1024); }
      bf16x8 h0 = __builtin_bit_cast(bf16x8, h[n][0]), h1 = __builtin_bit_cast(bf16x8, h[n][1]);
      acc[0][0] = mfma(wa, h0, acc[0][0]);
      acc[0][1] = mfma(wb, h0, acc[0][1]);
      acc[1][0] = mfma(wa, h1, acc[1][0]);
      acc[1][1] = mfma(wb, h1, acc[1][1]);
      if (n + 8 < 14) { h[n + 8][0] = ldsc(rws, lane * 16, hb + (n + 8) * 1024); h[n + 8][1] = ldsc(rws, lane * 16, hb + (112 + n + 8) * 1024); }
      SCHED();
    }
  }
  STAMP(4);
  {
    unsigned fo = lane < 4 ? W_FK + (w * 4 + lane) * 128 : W_F1 + (p2 * 16 + 14 + (w >> 2)) * 128;
    wait_flag(ws, fo, tag);
    STAMP(5);
    u32x4 kf[2][2], pq[2][2][2];
#pragma unroll
    for (int hd = 0; hd < 2; ++hd)
#pragma unroll
      for (int s = 0; s < 2; ++s) {
        kf[hd][s] = ldsc(rws, lane * 16, W_KVF + ((w * 2 + hd) * 2 + s) * 1024);
        pq[hd][0][s] = ldsc(rws, lane * 16, W_PQ + (((T * 2) * 16 + w * 2 + hd) * 2 + s) * 1024);
        pq[hd][1][s] = ldsc(rws, lane * 16, W_PQ + (((T * 2 + 1) * 16 + w * 2 + hd) * 2 + s) * 1024);
      }
#pragma unroll
    for (int hd = 0; hd < 2; ++hd) {
      bf16x8 wf[2][2];
#pragma unroll
      for (int s = 0; s < 2; ++s) {
        const int n = 5 + hd * 2 + s;
        wf[s][0] = ldtr(a20 + (n / 3) * STG + (n % 3) * 2048, 1024);
        wf[s][1] = ldtr(a21 + (n / 3) * STG + (n % 3) * 2048, 1024);
      }
#pragma unroll
      for (int th = 0; th < 2; ++th) {
        f32x16 nm;
#pragma unroll
        for (int r = 0; r < 16; ++r) nm[r] = 0.f;
        nm = mfma(__builtin_bit_cast(bf16x8, kf[hd][0]), __builtin_bit_cast(bf16x8, pq[hd][th][0]), nm);
        nm = mfma(__builtin_bit_cast(bf16x8, kf[hd][1]), __builtin_bit_cast(bf16x8, pq[hd][th][1]), nm);
        float rd = __builtin_amdgcn_rcpf(bcast_hi(nm[12]) + 1e-6f);
        float o[16];
#pragma unroll
        for (int r = 0; r < 16; ++r) o[r] = (r >= 12 && hh) ? 0.f : nm[r] * rd;
#pragma unroll
        for (int s = 0; s < 2; ++s) {
          u32x4 po = {pk2(o[8 * s], o[8 * s + 1]), pk2(o[8 * s + 2], o[8 * s + 3]), pk2(o[8 * s + 4], o[8 * s + 5]), pk2(o[8 * s + 6], o[8 * s + 7])};
          acc[th][0] = mfma(wf[s][0], __builtin_bit_cast(bf16x8, po), acc[th][0]);
          acc[th][1] = mfma(wf[s][1], __builtin_bit_cast(bf16x8, po), acc[th][1]);
        }
      }
    }
  }
  STAMP(9);
  WAITL0();
  BAR();
  STAMP(10);
#pragma unroll
  for (int th = 0; th < 2; ++th)
#pragma unroll
    for (int cb = 0; cb < 2; ++cb)
#pragma unroll
      for (int q = 0; q < 4; ++q) {
        f32x4 t = {acc[th][cb][4 * q], acc[th][cb][4 * q + 1], acc[th][cb][4 * q + 2], acc[th][cb][4 * q + 3]};
        *(LAS f32x4*)(w * 17408 + (th * 32 + l32) * 272 + (cb * 32 + q * 8 + hh * 4) * 4) = t;
      }
  WAITL0();
  BAR();
  STAMP(11);
  {
    unsigned a = (tid >> 3) * 272 + (tid & 7) * 32;
    f32x4 s0 = ldf4(a), s1 = ldf4(a + 16);
#pragma unroll
    for (int p = 1; p < 8; ++p) { s0 += ldf4(a + p * 17408); s1 += ldf4(a + p * 17408 + 16); }
    float r[8];
#pragma unroll
    for (int i = 0; i < 4; ++i) {
      float lo = __uint_as_float(xin[i] << 16) + __uint_as_float(bin[i] << 16), hi = __uint_as_float(xin[i] & 0xffff0000u) + __uint_as_float(bin[i] & 0xffff0000u);
      r[2 * i] = lo + (i < 2 ? s0[2 * i] : s1[2 * i - 4]);
      r[2 * i + 1] = hi + (i < 2 ? s0[2 * i + 1] : s1[2 * i - 3]);
    }
    u32x4 o = {pk2(r[0], r[1]), pk2(r[2], r[3]), pk2(r[4], r[5]), pk2(r[6], r[7])};
    *(GAS u32x4*)(out + (otok * 448 + ocol) * 2) = o;
  }
  STAMP(6); STAMP_OUT();
}

__global__ void prk(char* ws) {
  __shared__ unsigned long long t0s[256];
  __shared__ float acc[3][16][2];
  __shared__ float sd[256][16];
  unsigned b = threadIdx.x;
  unsigned long long st = __builtin_amdgcn_s_memrealtime();
  const unsigned long long* s = (const unsigned long long*)(ws + W_DBG);
  unsigned long long m = ~0ull; for (int w = 0; w < 8; ++w) m = min(m, s[(b * 8 + w) * 16]);
  t0s[b] = m; __syncthreads();
  unsigned long long t0 = ~0ull; for (int i = 0; i < 256; ++i) t0 = min(t0, t0s[i]);
  for (int k = 0; k < 14; ++k) { unsigned long long v = s[(b * 8) * 16 + k]; sd[b][k] = v ? (float)(v - t0) : 0.f; }
  __syncthreads();
  if (b < 42) { int r = b / 14, k = b % 14; float a = 0, mx = 0; int c = 0; for (int i = 0; i < 256; ++i) if (ROLE(i) == r) { a += sd[i][k]; mx = fmaxf(mx, sd[i][k]); c++; } acc[r][k][0] = a / c; acc[r][k][1] = mx; }
  __syncthreads();
#define A(r,k) acc[r][k][0]
#define M(r,k) acc[r][k][1]
#define MA(k) fmaxf(fmaxf(M(0,k),M(1,k)),M(2,k))
  float gap = (float)(long long)(st - t0) - MA(6);
  float ck = (s[15] - s[14]) / (float)(s[6] - s[0]) * 100.f;
  float v = k2 0.f MA(6) MA(0) A(0,7) A(0,1) A(0,2);
  v = fminf(fmaxf(v, 0.f), 10000.f);
  unsigned long long n = (unsigned long long)(v * 100.f);
  if (b == 0) { for (int i = 0; i < 100000000; ++i) { if (__builtin_amdgcn_s_memrealtime() - st >= n) break; __builtin_amdgcn_s_sleep(8); } }
}
extern "C" void kernel_launch(void* const* d_in, const int* in_sizes, int n_in, void* d_out, int out_size, void* d_ws, size_t ws_size,
                              hipStream_t stream) {
  (void)hipFuncSetAttribute((const void*)fused_kernel, hipFuncAttributeMaxDynamicSharedMemorySize, LDS_BYTES);
  fused_kernel<<<256, 512, LDS_BYTES, stream>>>((const char*)d_in[0], (const char*)d_in[1], (const char*)d_in[2], (const char*)d_in[4],
                                                (const char*)d_in[5], (const char*)d_in[6], (const char*)d_in[7], (char*)d_out, (char*)d_ws);
  prk<<<1,256,0,stream>>>((char*)d_ws);
}
```

```cpp
#include <hip/hip_runtime.h>
#pragma clang diagnostic ignored "-Wint-to-pointer-cast"

typedef unsigned long long u64;
typedef __attribute__((ext_vector_type(8))) __bf16 bf16x8;
typedef __attribute__((ext_vector_type(2))) __bf16 bf16x2;
typedef __attribute__((ext_vector_type(4))) short s16x4;
typedef __attribute__((ext_vector_type(16))) float f32x16;
typedef __attribute__((ext_vector_type(4))) float f32x4;
typedef __attribute__((ext_vector_type(2))) float f32x2;
typedef __attribute__((ext_vector_type(4))) unsigned u32x4;
typedef __attribute__((ext_vector_type(2))) unsigned u32x2;
typedef __amdgpu_buffer_rsrc_t rsrc_t;
#define GAS __attribute__((address_space(1)))
#define LAS __attribute__((address_space(3)))
#define DI __device__ __forceinline__
#define SCHED() __builtin_amdgcn_sched_barrier(0)
#define BAR() __builtin_amdgcn_s_barrier()
#define WAITV(n) asm volatile("s_waitcnt vmcnt(" #n ")" ::: "memory")
#define WAITL0() asm volatile("s_waitcnt lgkmcnt(0)" ::: "memory")

constexpr unsigned STG = 51456;
constexpr unsigned XOFF = 34816;
constexpr unsigned L_CSP = 154368;
constexpr unsigned L_ST = 158464;
constexpr unsigned L_CSF = 159488;
constexpr unsigned L_RS = 161536;
constexpr unsigned LDS_BYTES = 163840;
constexpr unsigned W_GEN = 0;
constexpr unsigned W_F1 = 4096;
constexpr unsigned W_FK = W_F1 + 256 * 128;
constexpr unsigned W_KVF = 65536;
constexpr unsigned W_KVP = 131072;
constexpr unsigned W_PQ = W_KVP + (1u << 20);
constexpr unsigned W_H = W_PQ + (2u << 20);
constexpr unsigned W_END = W_H + 64u * 112u * 1024u;
constexpr unsigned W_DBG = 16u << 20;
constexpr int SPIN_MAX = 400000;

#define STAMP(i) do {} while (0)
#define STAMP_OUT() do {} while (0)
#define CYC(i) do {} while (0)

DI void glds(unsigned ldsaddr, unsigned voff, rsrc_t r, unsigned soff) {
  asm volatile("s_mov_b32 m0, %0\n\ts_nop 0\n\tbuffer_load_dwordx4 %1, %2, %3 offen lds" ::"s"(ldsaddr), "v"(voff), "s"(r), "s"(soff) : "memory");
}
DI bf16x8 ld128(unsigned a) { return *(LAS bf16x8*)a; }
DI f32x4 ldf4(unsigned a) { return *(LAS f32x4*)a; }
DI bf16x8 ldtr(unsigned a, unsigned hi) {
  s16x4 lo = __builtin_amdgcn_ds_read_tr16_b64_v4i16((LAS s16x4*)a);
  s16x4 h = __builtin_amdgcn_ds_read_tr16_b64_v4i16((LAS s16x4*)(a + hi));
  return __builtin_bit_cast(bf16x8, __builtin_shufflevector(lo, h, 0, 1, 2, 3, 4, 5, 6, 7));
}
DI f32x16 mfma(bf16x8 a, bf16x8 b, f32x16 c) { return __builtin_amdgcn_mfma_f32_32x32x16_bf16(a, b, c, 0, 0, 0); }
DI float dot2(unsigned a, unsigned b, float c) { return __builtin_amdgcn_fdot2_f32_bf16(__builtin_bit_cast(bf16x2, a), __builtin_bit_cast(bf16x2, b), c, false); }
DI unsigned pk2(float a, float b) { f32x2 v = {a, b}; return __builtin_bit_cast(unsigned, __builtin_convertvector(v, bf16x2)); }
DI float halfsum(float x) { auto t = __builtin_amdgcn_permlane32_swap(__float_as_uint(x), __float_as_uint(x), false, false); return __uint_as_float(t[0]) + __uint_as_float(t[1]); }
DI float bcast_hi(float x) { auto t = __builtin_amdgcn_permlane32_swap(__float_as_uint(x), __float_as_uint(x), false, false); return __uint_as_float(t[1]); }
DI u32x4 ldsc(rsrc_t r, unsigned voff, unsigned soff) { return __builtin_amdgcn_raw_buffer_load_b128(r, voff, soff, 16); }
DI void stsc(u32x4 v, rsrc_t r, unsigned voff, unsigned soff) { __builtin_amdgcn_raw_buffer_store_b128(v, r, voff, soff, 16); }
DI float phi(float t) { return (t > 0.f ? t + 1.f : __builtin_amdgcn_exp2f(t * 1.4426950408889634f)) + 1e-6f; }
DI void wait_flag(const char* ws, unsigned off, u64 tag) {
  for (int s = 0; s < SPIN_MAX; ++s) {
    u64 v = __hip_atomic_load((GAS u64*)(ws + off), __ATOMIC_RELAXED, __HIP_MEMORY_SCOPE_AGENT);
    if (__all(v == tag)) break;
    __builtin_amdgcn_s_sleep(1);
  }
  __builtin_amdgcn_fence(__ATOMIC_ACQUIRE, "wavefront");
}

struct Ld {
  rsrc_t rx, rw, r2, ro;
  unsigned vx, vw, v2, v2b;
  unsigned rs;
  unsigned c0;
  unsigned w;
};
DI void issue_chunk(const Ld& L, unsigned i, unsigned sb) {
  unsigned c = L.c0 + i; c = c >= 7 ? c - 7 : c;
  unsigned sx = c * 128, sw = c * 64 * L.rs;
  unsigned lx = sb + XOFF + L.w * 2080, lw = sb + L.w * 4352;
  glds(lx, L.vx, L.rx, sx);
  glds(lw, L.vw, L.rw, sw);
  glds(lw + 1088, L.vw, L.rw, sw + 2 * L.rs);
  glds(lx + 1040, L.vx, L.rx, sx + 7168);
  glds(lw + 2176, L.vw, L.rw, sw + 4 * L.rs);
  glds(lw + 3264, L.vw, L.rw, sw + 6 * L.rs);
}
DI void issue_w2_h(const Ld& L, unsigned gi, unsigned reg) {
  unsigned l = reg * STG + L.w * 6144, s = (L.w * 224 + gi * 48) * 896;
#pragma unroll
  for (int j = 0; j < 6; ++j) glds(l + j * 1024, L.v2, L.r2, s + j * 7168);
}
DI void issue_w2_g4(const Ld& L) {
  unsigned l = STG + L.w * 6144, s = (L.w * 224 + 192) * 896, so = L.w * 56 * 896;
#pragma unroll
  for (int j = 0; j < 4; ++j) glds(l + j * 1024, L.v2, L.r2, s + j * 7168);
  glds(l + 4096, L.v2, L.ro, so);
  glds(l + 5120, L.v2, L.ro, so + 7168);
}
DI void issue_w2_g5(const Ld& L) {
  unsigned l = 2 * STG + L.w * 6144, so = L.w * 56 * 896;
  glds(l, L.v2, L.ro, so + 2 * 7168);
  glds(l + 1024, L.v2b, L.ro, so + 3 * 7168);
  so += 28 * 896;
  glds(l + 2048, L.v2, L.ro, so);
  glds(l + 3072, L.v2, L.ro, so + 7168);
  glds(l + 4096, L.v2, L.ro, so + 2 * 7168);
  glds(l + 5120, L.v2b, L.ro, so + 3 * 7168);
}

template <int NB> struct Frag { bf16x8 x; bf16x8 w[NB]; };
template <int NB, int S> DI void ld_frag(Frag<NB>& f, unsigned sb, const unsigned (&wa)[NB], unsigned xa0, unsigned xa1) {
  f.x = ld128(sb + (S < 2 ? xa0 : xa1) + (S & 1) * 32);
#pragma unroll
  for (int b = 0; b < NB; ++b) f.w[b] = ldtr(sb + wa[b] + S * 8704, 2176);
}
struct G6 { rsrc_t ra, rb; unsigned va, vb, la, lb, sa, sb, dla, dlb, dsb; };
template <int K> DI void g6_issue(const G6& g) {
  if (K == 0) glds(g.la, g.va, g.ra, g.sa);
  else if (K == 3) glds(g.la + g.dla, g.va, g.ra, g.sa + 7168);
  else { constexpr int j = K < 3 ? K - 1 : K - 2; glds(g.lb + j * g.dlb, g.vb, g.rb, g.sb + j * g.dsb); }
}
template <int NB, bool TR, bool SQ, int SN, int G>
DI void step(const Frag<NB>& f, Frag<NB>& n, unsigned sn, const unsigned (&wa)[NB], unsigned xa0, unsigned xa1, const G6& g6,
             f32x16 (&acc)[NB], float (&cs)[NB], float (&st)[2], bool docs) {
  const u32x4 xv = __builtin_bit_cast(u32x4, f.x);
#pragma unroll
  for (int b = 0; b < NB; ++b) {
    acc[b] = TR ? mfma(f.w[b], f.x, acc[b]) : mfma(f.x, f.w[b], acc[b]);
    SCHED();
    if (SN >= 0) {
      if (b == 0) n.x = ld128(sn + (SN < 2 ? xa0 : xa1) + (SN & 1) * 32);
      if (2 * b < NB) n.w[2 * b] = ldtr(sn + wa[2 * b] + SN * 8704, 2176);
      if (2 * b + 1 < NB) n.w[2 * b + 1] = ldtr(sn + wa[2 * b + 1] + SN * 8704, 2176);
    }
    if (b >= NB - 2) {
      constexpr int NB2 = NB - 2;
      st[0] = dot2(xv[2 * (b - NB2)], SQ ? xv[2 * (b - NB2)] : 0x3f803f80u, st[0]);
      st[1] = dot2(xv[2 * (b - NB2) + 1], SQ ? xv[2 * (b - NB2) + 1] : 0x3f803f80u, st[1]);
      asm volatile("" : "+v"(st[0]), "+v"(st[1]));
    }
    if (G >= 0 && b == NB - 2) g6_issue<G < 0 ? 0 : G>(g6);
    if (G >= 0 && b == NB - 1) g6_issue<G < 0 ? 0 : G + 1>(g6);
    SCHED();
  }
  if (docs) {
#pragma unroll
    for (int j = 0; j < 4; ++j)
#pragma unroll
      for (int b = 0; b < NB; ++b) cs[b] = dot2(__builtin_bit_cast(u32x4, f.w[b])[j], 0x3f803f80u, cs[b]);
  }
  SCHED();
}
template <int NB, bool TR, bool SQ>
DI void kloop(f32x16 (&acc)[NB], const unsigned (&wa)[NB], unsigned xa0, unsigned xa1, const Ld& L, unsigned b0, unsigned lane) {
  float cs[NB]; float st[2] = {0.f, 0.f};
  const unsigned g = L.w & 3;
#pragma unroll
  for (int b = 0; b < NB; ++b) { cs[b] = 0.f; for (int r = 0; r < 16; ++r) acc[b][r] = 0.f; }
  Frag<NB> f0, f1;
  unsigned sb = 2 * STG, fb = STG;
  ld_frag<NB, 0>(f0, sb, wa, xa0, xa1);
#pragma unroll 1
  for (unsigned i = 0; i < 7; ++i) {
    G6 g6;
    {
      const bool k = i < 5;
      unsigned c = L.c0 + i + 2; c = c >= 7 ? c - 7 : c;
      const unsigned s2 = (L.w * 224 + (i - 5) * 48) * 896, l2 = fb + L.w * 6144;
      g6.ra = k ? L.rx : L.r2; g6.rb = k ? L.rw : L.r2;
      g6.va = k ? L.vx : L.v2; g6.vb = k ? L.vw : L.v2;
      g6.la = k ? fb + XOFF + L.w * 2080 : l2; g6.lb = k ? fb + L.w * 4352 : l2 + 2048;
      g6.sa = k ? c * 128 : s2; g6.sb = k ? c * 64 * L.rs : s2 + 2 * 7168;
      g6.dla = k ? 1040 : 1024; g6.dlb = k ? 1088 : 1024; g6.dsb = k ? 2 * L.rs : 7168;
    }
    const unsigned nb = sb == 2 * STG ? 0 : sb + STG;
    SCHED();
    step<NB, TR, SQ, 1, 0>(f0, f1, sb, wa, xa0, xa1, g6, acc, cs, st, g == 0);
    step<NB, TR, SQ, 2, 2>(f1, f0, sb, wa, xa0, xa1, g6, acc, cs, st, g == 1);
    step<NB, TR, SQ, 3, 4>(f0, f1, sb, wa, xa0, xa1, g6, acc, cs, st, g == 2);
    __builtin_amdgcn_s_waitcnt(0xC07F);
    WAITV(6);
    BAR();
    SCHED();
    step<NB, TR, SQ, 0, -1>(f1, f0, nb, wa, xa0, xa1, g6, acc, cs, st, g == 3);
    fb = sb; sb = nb;
  }
  SCHED();
  *(LAS float*)(L_ST + (SQ ? 512 : 0) + (g * 32 + (lane & 31)) * 4) = halfsum(st[0] + st[1]);
#pragma unroll
  for (int b = 0; b < NB; ++b) *(LAS float*)(L_CSP + (((b0 + b) * 4 + g) * 32 + (lane & 31)) * 4) = halfsum(cs[b]);
  WAITL0();
  BAR();
}
DI float colsum(unsigned blk, unsigned l32) {
  unsigned a = L_CSP + (blk * 128 + l32) * 4;
  return (*(LAS float*)a + *(LAS float*)(a + 128)) + (*(LAS float*)(a + 256) + *(LAS float*)(a + 384));
}
DI void rowstat(unsigned tok, float& mu, float& var) {
  float s1 = *(LAS float*)(L_ST + tok * 4), s2 = *(LAS float*)(L_ST + 512 + tok * 4);
  mu = s1 * (1.f / 448.f);
  var = fmaxf(s2 * (1.f / 448.f) - mu * mu, 0.f);
}
template <int N0, int N1> DI void w2_to_regs(bf16x8 (&w2r)[9][2], unsigned a20, unsigned a21) {
#pragma unroll
  for (int n = N0; n < N1; ++n) {
    w2r[n][0] = ldtr(a20 + (n / 3) * STG + (n % 3) * 2048, 1024);
    w2r[n][1] = ldtr(a21 + (n / 3) * STG + (n % 3) * 2048, 1024);
  }
}

template <bool GELU> DI void epi_tr(const f32x16& acc, unsigned blk, float a, float m, unsigned hh, rsrc_t rws, unsigned voff, unsigned soff) {
  float v[16];
#pragma unroll
  for (int q = 0; q < 4; ++q) {
    f32x4 c = ldf4(L_CSF + blk * 256 + q * 32 + hh * 16);
    f32x4 b = {0.f, 0.f, 0.f, 0.f};
    if (GELU) b = ldf4(L_CSF + blk * 256 + 128 + q * 32 + hh * 16);
#pragma unroll
    for (int j = 0; j < 4; ++j) {
      float u = fmaf(a, acc[q * 4 + j], fmaf(-m, c[j], b[j]));
      if (GELU) {
        float p = fmaf(u * u, -0.10294323970248637f, -2.302208198144325f);
        float e = __builtin_amdgcn_exp2f(u * p);
        v[q * 4 + j] = u * __builtin_amdgcn_rcpf(1.f + e);
      } else {
        float t = phi(u);
        v[q * 4 + j] = (q == 3 && hh) ? 0.f : t;
      }
    }
  }
  u32x4 p0 = {pk2(v[0], v[1]), pk2(v[2], v[3]), pk2(v[4], v[5]), pk2(v[6], v[7])};
  u32x4 p1 = {pk2(v[8], v[9]), pk2(v[10], v[11]), pk2(v[12], v[13]), pk2(v[14], v[15])};
  stsc(p0, rws, voff, soff);
  stsc(p1, rws, voff, soff + 1024);
}

template <int NB, bool SQ>
DI void unit_w1(const Ld& L, unsigned o0, unsigned b0, const float (&bias)[4], unsigned xa0, unsigned xa1, unsigned wl, unsigned par, unsigned lane,
                rsrc_t rws, unsigned hsoff, bf16x8 (&w2r)[9][2], unsigned a20, unsigned a21, u64 (&sv)[16]) {
  f32x16 acc[NB]; unsigned wa[NB];
#pragma unroll
  for (int b = 0; b < NB; ++b) wa[b] = wl + ((o0 + (b0 + b) * 64) ^ par);
  kloop<NB, true, SQ>(acc, wa, xa0, xa1, L, b0, lane);
  STAMP(1);
  issue_w2_h(L, 2, 2);
  const unsigned l32 = lane & 31, hh = lane >> 5;
#pragma unroll
  for (int b = 0; b < NB; ++b) {
    float c = colsum(b0 + b, l32);
    *(LAS float*)(L_CSF + (b0 + b) * 256 + lane * 4) = hh ? bias[b] : c;
  }
  float mu, var; rowstat((L.w & 3) * 32 + l32, mu, var);
  float r = __builtin_amdgcn_rsqf(var + 1e-5f);
  float a = r * __builtin_amdgcn_rsqf(var * r * r + 1e-5f), m = a * mu;
  WAITV(6);
  w2_to_regs<0, 6>(w2r, a20, a21);
  WAITL0();
  SCHED();
  STAMP(12);
  issue_w2_h(L, 3, 0);
  issue_w2_g4(L);
  STAMP(13);
#pragma unroll
  for (int b = 0; b < NB - 1; ++b) { epi_tr<true>(acc[b], b0 + b, a, m, hh, rws, lane * 16, hsoff + (b0 + b) * 2048); SCHED(); }
  if (NB == 4) WAITV(18); else WAITV(16);
  w2_to_regs<6, 9>(w2r, a20, a21);
  epi_tr<true>(acc[NB - 1], b0 + NB - 1, a, m, hh, rws, lane * 16, hsoff + (b0 + NB - 1) * 2048);
  WAITL0();
  SCHED();
  issue_w2_g5(L);
  STAMP(8);
  WAITV(6);
}
template <bool SQ>
DI void unit_q(const Ld& L, unsigned o0, unsigned b0, unsigned xa0, unsigned xa1, unsigned wl, unsigned par, unsigned lane, rsrc_t rws, unsigned qsoff, u64 (&sv)[16]) {
  f32x16 acc[4]; unsigned wa[4];
#pragma unroll
  for (int b = 0; b < 4; ++b) wa[b] = wl + ((o0 + (b0 + b) * 56) ^ par);
  kloop<4, true, SQ>(acc, wa, xa0, xa1, L, b0, lane);
  STAMP(1);
  const unsigned l32 = lane & 31, hh = lane >> 5;
#pragma unroll
  for (int b = 0; b < 4; ++b) {
    float c = colsum(b0 + b, l32);
    *(LAS float*)(L_CSF + (b0 + b) * 256 + lane * 4) = c;
  }
  float mu, var; rowstat((L.w & 3) * 32 + l32, mu, var);
  float a = __builtin_amdgcn_rsqf(var + 1e-5f), m = a * mu;
#pragma unroll
  for (int b = 0; b < 4; ++b) epi_tr<false>(acc[b], b0 + b, a, m, hh, rws, lane * 16, qsoff + (b0 + b) * 2048);
  STAMP(8);
  WAITV(0);
}
template <int NH, bool SQ>
DI void unit_kv(const Ld& L, unsigned o0, unsigned h0, unsigned xa0, unsigned xa1, unsigned wl, unsigned par, unsigned lane,
                bf16x8 (&w2r)[9][2], unsigned a20, unsigned a21, u64 (&sv)[16]) {
  f32x16 acc[2 * NH]; unsigned wa[2 * NH];
#pragma unroll
  for (int b = 0; b < 2 * NH; ++b) wa[b] = wl + ((o0 + (h0 + (b >> 1)) * 56 + (b & 1) * 256) ^ par);
  kloop<2 * NH, false, SQ>(acc, wa, xa0, xa1, L, 2 * h0, lane);
  STAMP(1);
  const unsigned l32 = lane & 31, hh = lane >> 5, g = L.w & 3;
  float mu, var; rowstat(g * 32 + l32, mu, var);
  float r = __builtin_amdgcn_rsqf(var + 1e-5f);
  *(LAS float*)(L_RS + hh * 512 + (g * 32 + l32) * 4) = hh ? r * mu : r;
  float cs[2 * NH];
#pragma unroll
  for (int b = 0; b < 2 * NH; ++b) cs[b] = colsum(2 * h0 + b, l32);
  WAITV(0);
  w2_to_regs<0, 6>(w2r, a20, a21);
  WAITL0();
  SCHED();
  issue_w2_h(L, 3, 0);
  issue_w2_g4(L);
  f32x4 ra[4], rm[4];
#pragma unroll
  for (int q = 0; q < 4; ++q) { ra[q] = ldf4(L_RS + (g * 32 + q * 8 + hh * 4) * 4); rm[q] = ldf4(L_RS + 512 + (g * 32 + q * 8 + hh * 4) * 4); }
  const float vpad = l32 == 28 ? 1.f : 0.f;
#pragma unroll
  for (int h = 0; h < NH; ++h) {
    float k[16], v[16];
#pragma unroll
    for (int i = 0; i < 16; ++i) {
      float kk = phi(fmaf(ra[i >> 2][i & 3], acc[2 * h][i], -rm[i >> 2][i & 3] * cs[2 * h]));
      float vv = fmaf(ra[i >> 2][i & 3], acc[2 * h + 1][i], -rm[i >> 2][i & 3] * cs[2 * h + 1]);
      k[i] = l32 < 28 ? kk : 0.f;
      v[i] = l32 < 28 ? vv : vpad;
    }
    f32x16 kv;
#pragma unroll
    for (int i = 0; i < 16; ++i) kv[i] = 0.f;
#pragma unroll
    for (int s = 0; s < 2; ++s) {
      u32x4 pk = {pk2(k[8 * s], k[8 * s + 1]), pk2(k[8 * s + 2], k[8 * s + 3]), pk2(k[8 * s + 4], k[8 * s + 5]), pk2(k[8 * s + 6], k[8 * s + 7])};
      u32x4 pv = {pk2(v[8 * s], v[8 * s + 1]), pk2(v[8 * s + 2], v[8 * s + 3]), pk2(v[8 * s + 4], v[8 * s + 5]), pk2(v[8 * s + 6], v[8 * s + 7])};
      kv = mfma(__builtin_bit_cast(bf16x8, pk), __builtin_bit_cast(bf16x8, pv), kv);
    }
#pragma unroll
    for (int q = 0; q < 4; ++q) {
      f32x4 t = {kv[4 * q], kv[4 * q + 1], kv[4 * q + 2], kv[4 * q + 3]};
      *(LAS f32x4*)(2 * STG + (((h0 + h) * 4 + g) * 4 + q) * 1024 + lane * 16) = t;
    }
  }
  WAITL0();
}

__global__ __launch_bounds__(512, 2) void fused_kernel(const char* __restrict__ x, const char* __restrict__ wqkv, const char* __restrict__ wo,
                                                       const char* __restrict__ w1, const char* __restrict__ b1, const char* __restrict__ w2,
                                                       const char* __restrict__ b2, char* __restrict__ out, char* ws) {
  extern __shared__ __attribute__((aligned(16))) char lds_dyn[];
  const unsigned tid = threadIdx.x, lane = tid & 63, w = __builtin_amdgcn_readfirstlane(tid >> 6);
  const unsigned xcd = blockIdx.x & 7, bi = blockIdx.x >> 3;
  u64 sv[16] = {0, 0, 0, 0, 0, 0, 0, 0, 0, 0, 0, 0, 0, 0, 0, 0};
  CYC(14);
  STAMP(0);
  const unsigned panel = (xcd & 3) * 4 + (bi >> 3), j = bi & 7;
  const bool is_w1 = xcd < 4, is_q = !is_w1 && j >= 6;
  const unsigned slot = is_w1 ? j : 8 + j;
  unsigned u2 = is_w1 ? xcd * 32 + bi : 128 + (xcd - 4) * 24 + (bi >> 3) * 6 + j;
  if (is_q) u2 = 0;
  const unsigned T = u2 / 7, ct = u2 - T * 7;
  unsigned seg0, seg1, o0;
  const unsigned kvh0 = j < 2 ? 3 * j : (j < 4 ? 2 * j + 2 : 3 * j - 2);
  if (is_w1) { seg0 = (448 * j) & ~127u; seg1 = seg0 + 256; o0 = (j & 1) * 64; }
  else if (is_q) { seg0 = (j - 6) * 384; seg1 = seg0 + 256; o0 = (j - 6) * 64; }
  else { seg0 = 896 + 128 * j; seg1 = 1792 + 128 * j; o0 = 56 * kvh0 - 128 * j; }

  Ld L;
  L.w = w;
  L.rs = is_w1 ? 3584 : 2688;
#define ROT 0
  L.c0 = ROT == 1 ? (panel * 2 + j) % 7 : (ROT == 2 ? (panel & 2) * 2 : (ROT == 3 ? ((panel & 3) * 7) / 4 : 0));
  L.rx = __builtin_amdgcn_make_buffer_rsrc((void*)(x + panel * 128 * 896), 0, 128 * 896, 0x00020000);
  L.rw = __builtin_amdgcn_make_buffer_rsrc((void*)(is_w1 ? w1 : wqkv), 0, 448 * L.rs, 0x00020000);
  L.r2 = __builtin_amdgcn_make_buffer_rsrc((void*)w2, 0, 1792 * 896, 0x00020000);
  L.ro = __builtin_amdgcn_make_buffer_rsrc((void*)wo, 0, 448 * 896, 0x00020000);
  const rsrc_t rws = __builtin_amdgcn_make_buffer_rsrc((void*)ws, 0, W_END, 0x00020000);
  {
    unsigned r8 = lane >> 3, c = ((lane & 7) ^ ((lane >> 2) & 4)) * 16;
    L.vx = (w * 16 + r8) * 896 + c;
    L.v2 = r8 * 896 + c + ct * 128;
    L.v2b = (r8 & 3) * 896 + c + ct * 128;
    unsigned rp = lane >> 5, sp = ((lane & 31) * 16) ^ (rp * 128);
    L.vw = (w * 8 + rp) * L.rs + ((sp & 256) ? seg1 : seg0) + (sp & 255);
  }
  const unsigned grp = w >> 2, b0w = grp * 4;
  u32x2 gen; unsigned bb[4];
  {
    unsigned zero = 0, bo = is_w1 ? (224 * j + b0w * 32 + (lane & 31)) * 2 : 0;
    unsigned bo3 = grp ? bo : bo + 192;
    asm volatile("s_nop 4\n\tglobal_load_dwordx2 %0, %5, %6 sc1\n\tglobal_load_ushort %1, %7, %8\n\tglobal_load_ushort %2, %7, %8 offset:64\n\t"
                 "global_load_ushort %3, %7, %8 offset:128\n\tglobal_load_ushort %4, %9, %8"
                 : "=&v"(gen), "=&v"(bb[0]), "=&v"(bb[1]), "=&v"(bb[2]), "=&v"(bb[3]) : "v"(zero), "s"(ws), "v"(bo), "s"(b1), "v"(bo3) : "memory");
  }
  issue_chunk(L, 0, 2 * STG);
  issue_chunk(L, 1, 0);
  const unsigned l32 = lane & 31, hh = lane >> 5, q4 = (lane & 15) >> 2, blk = (lane >> 4) & 1, p4 = lane & 3;
  unsigned xa0, xa1;
  {
    unsigned r = (w & 3) * 32 + l32, f = (r >> 1) & 1, base = XOFF + (r >> 3) * 1040 + (r & 7) * 128 + hh * 16;
    xa0 = base + f * 64; xa1 = base + (f ^ 1) * 64;
  }
  const unsigned par = (q4 & 1) * 128, wl = 1088 * (4 * hh + (q4 >> 1)) + (q4 & 1) * 512;
  o0 += blk * 32 + p4 * 8;
  const unsigned a20 = (4 * hh + q4) * 128 + (q4 >> 1) * 64 + blk * 32 + p4 * 8 + w * 6144, a21 = a20 ^ 64;
  asm volatile("s_waitcnt vmcnt(6)" : "+v"(gen), "+v"(bb[0]), "+v"(bb[1]), "+v"(bb[2]), "+v"(bb[3])::"memory");
  const u64 g0 = ((u64)(unsigned)__builtin_amdgcn_readfirstlane(gen[1]) << 32) | (unsigned)__builtin_amdgcn_readfirstlane(gen[0]);
  const u64 tag = g0 * 6364136223846793005ull + 1442695040888963407ull;
  BAR();
  STAMP(7);

  bf16x8 w2r[9][2];
  if (is_w1) {
    float bias[4];
#pragma unroll
    for (int b = 0; b < 4; ++b) bias[b] = __uint_as_float(bb[b] << 16);
    unsigned hsoff = W_H + ((panel * 4 + (w & 3)) * 112 + j * 14) * 1024;
    if (grp == 0) unit_w1<4, false>(L, o0, 0, bias, xa0, xa1, wl, par, lane, rws, hsoff, w2r, a20, a21, sv);
    else unit_w1<3, true>(L, o0, 4, bias, xa0, xa1, wl, par, lane, rws, hsoff, w2r, a20, a21, sv);
    BAR();
    if (tid == 0) __hip_atomic_store((GAS u64*)(ws + W_F1 + (panel * 16 + slot) * 128), tag, __ATOMIC_RELAXED, __HIP_MEMORY_SCOPE_AGENT);
  } else if (is_q) {
    unsigned qsoff = W_PQ + ((panel * 4 + (w & 3)) * 16 + (j - 6) * 8) * 2048;
    if (grp == 0) unit_q<false>(L, o0, 0, xa0, xa1, wl, par, lane, rws, qsoff, sv);
    else unit_q<true>(L, o0, 4, xa0, xa1, wl, par, lane, rws, qsoff, sv);
    BAR();
    if (tid == 0) __hip_atomic_store((GAS u64*)(ws + W_F1 + (panel * 16 + slot) * 128), tag, __ATOMIC_RELAXED, __HIP_MEMORY_SCOPE_AGENT);
    STAMP(2);
    const unsigned rj = panel * 2 + (j - 6), head = rj >> 1, sh = rj & 1;
    if (w == 0) {
      const unsigned gj = head < 6 ? head / 3 : (head < 10 ? (head - 2) >> 1 : (head + 2) / 3);
      wait_flag(ws, W_F1 + ((lane & 15) * 16 + 8 + gj) * 128, tag);
      STAMP(3);
      u32x4 t[16][2];
#pragma unroll
      for (int p = 0; p < 16; ++p) {
        t[p][0] = ldsc(rws, lane * 16, W_KVP + (p * 16 + head) * 4096 + sh * 2048);
        t[p][1] = ldsc(rws, lane * 16, W_KVP + (p * 16 + head) * 4096 + sh * 2048 + 1024);
      }
      f32x4 s0 = __builtin_bit_cast(f32x4, t[0][0]), s1 = __builtin_bit_cast(f32x4, t[0][1]);
#pragma unroll
      for (int p = 1; p < 16; ++p) { s0 += __builtin_bit_cast(f32x4, t[p][0]); s1 += __builtin_bit_cast(f32x4, t[p][1]); }
      u32x4 o = {pk2(s0[0], s0[1]), pk2(s0[2], s0[3]), pk2(s1[0], s1[1]), pk2(s1[2], s1[3])};
      stsc(o, rws, lane * 16, W_KVF + rj * 1024);
      WAITV(0);
      if (lane == 0) __hip_atomic_store((GAS u64*)(ws + W_FK + rj * 128), tag, __ATOMIC_RELAXED, __HIP_MEMORY_SCOPE_AGENT);
      STAMP(4);
    } else if (w == 1 && rj == 0) {
#pragma unroll 1
      for (int k = 0; k < 4; ++k) wait_flag(ws, W_F1 + (k * 64 + lane) * 128, tag);
      if (lane == 0) __hip_atomic_store((GAS u64*)(ws + W_GEN), tag, __ATOMIC_RELAXED, __HIP_MEMORY_SCOPE_AGENT);
    }
    STAMP(6); STAMP_OUT();
    return;
  } else {
    const bool three = j < 2 || j >= 4;
    if (grp == 0) {
      if (three) unit_kv<2, false>(L, o0, 0, xa0, xa1, wl, par, lane, w2r, a20, a21, sv);
      else unit_kv<1, false>(L, o0, 0, xa0, xa1, wl, par, lane, w2r, a20, a21, sv);
    } else unit_kv<1, true>(L, o0, three ? 2 : 1, xa0, xa1, wl, par, lane, w2r, a20, a21, sv);
    BAR();
    for (unsigned it = w; it < (three ? 12u : 8u); it += 8) {
      unsigned a = 2 * STG + ((it >> 2) * 16 + (it & 3)) * 1024 + lane * 16;
      f32x4 s = (ldf4(a) + ldf4(a + 4096)) + (ldf4(a + 8192) + ldf4(a + 12288));
      stsc(__builtin_bit_cast(u32x4, s), rws, lane * 16, W_KVP + (panel * 16 + kvh0) * 4096 + it * 1024);
    }
    WAITV(0);
    BAR();
    if (tid == 0) __hip_atomic_store((GAS u64*)(ws + W_F1 + (panel * 16 + slot) * 128), tag, __ATOMIC_RELAXED, __HIP_MEMORY_SCOPE_AGENT);
    issue_w2_h(L, 2, 2);
    WAITV(0);
    w2_to_regs<6, 9>(w2r, a20, a21);
    WAITL0();
    SCHED();
    issue_w2_g5(L);
  }
  STAMP(2);

  const unsigned p2 = T >> 1;
  const unsigned otok = T * 64 + (tid >> 3), ocol = ct * 64 + (tid & 7) * 8;
  const u32x4 xin = *(const GAS u32x4*)(x + (otok * 448 + ocol) * 2);
  const u32x4 bin = *(const GAS u32x4*)(b2 + ocol * 2);
  wait_flag(ws, W_F1 + (p2 * 16 + w) * 128, tag);
  STAMP(3);
  f32x16 acc[2][2];
#pragma unroll
  for (int i = 0; i < 4; ++i)
#pragma unroll
    for (int r = 0; r < 16; ++r) acc[i >> 1][i & 1][r] = 0.f;
  {
    const unsigned hb = W_H + (T * 2 * 112 + w * 14) * 1024;
    u32x4 h[14][2];
#pragma unroll
    for (int n = 0; n < 8; ++n) { h[n][0] = ldsc(rws, lane * 16, hb + n * 1024); h[n][1] = ldsc(rws, lane * 16, hb + (112 + n) * 1024); }
    SCHED();
#pragma unroll
    for (int n = 0; n < 14; ++n) {
      bf16x8 wa, wb;
      if (n < 9) { wa = w2r[n][0]; wb = w2r[n][1]; }
      else { wa = ldtr(a20 + ((n - 9) / 3) * STG + ((n - 9) % 3) * 2048, 1024); wb = ldtr(a21 + ((n - 9) / 3) * STG + ((n - 9) % 3) * 2048, 1024); }
      bf16x8 h0 = __builtin_bit_cast(bf16x8, h[n][0]), h1 = __builtin_bit_cast(bf16x8, h[n][1]);
      acc[0][0] = mfma(wa, h0, acc[0][0]);
      acc[0][1] = mfma(wb, h0, acc[0][1]);
      acc[1][0] = mfma(wa, h1, acc[1][0]);
      acc[1][1] = mfma(wb, h1, acc[1][1]);
      if (n + 8 < 14) { h[n + 8][0] = ldsc(rws, lane * 16, hb + (n + 8) * 1024); h[n + 8][1] = ldsc(rws, lane * 16, hb + (112 + n + 8) * 1024); }
      SCHED();
    }
  }
  STAMP(4);
  {
    unsigned fo = lane < 4 ? W_FK + (w * 4 + lane) * 128 : W_F1 + (p2 * 16 + 14 + (w >> 2)) * 128;
    wait_flag(ws, fo, tag);
    STAMP(5);
    u32x4 kf[2][2], pq[2][2][2];
#pragma unroll
    for (int hd = 0; hd < 2; ++hd)
#pragma unroll
      for (int s = 0; s < 2; ++s) {
        kf[hd][s] = ldsc(rws, lane * 16, W_KVF + ((w * 2 + hd) * 2 + s) * 1024);
        pq[hd][0][s] = ldsc(rws, lane * 16, W_PQ + (((T * 2) * 16 + w * 2 + hd) * 2 + s) * 1024);
        pq[hd][1][s] = ldsc(rws, lane * 16, W_PQ + (((T * 2 + 1) * 16 + w * 2 + hd) * 2 + s) * 1024);
      }
#pragma unroll
    for (int hd = 0; hd < 2; ++hd) {
      bf16x8 wf[2][2];
#pragma unroll
      for (int s = 0; s < 2; ++s) {
        const int n = 5 + hd * 2 + s;
        wf[s][0] = ldtr(a20 + (n / 3) * STG + (n % 3) * 2048, 1024);
        wf[s][1] = ldtr(a21 + (n / 3) * STG + (n % 3) * 2048, 1024);
      }
#pragma unroll
      for (int th = 0; th < 2; ++th) {
        f32x16 nm;
#pragma unroll
        for (int r = 0; r < 16; ++r) nm[r] = 0.f;
        nm = mfma(__builtin_bit_cast(bf16x8, kf[hd][0]), __builtin_bit_cast(bf16x8, pq[hd][th][0]), nm);
        nm = mfma(__builtin_bit_cast(bf16x8, kf[hd][1]), __builtin_bit_cast(bf16x8, pq[hd][th][1]), nm);
        float rd = __builtin_amdgcn_rcpf(bcast_hi(nm[12]) + 1e-6f);
        float o[16];
#pragma unroll
        for (int r = 0; r < 16; ++r) o[r] = (r >= 12 && hh) ? 0.f : nm[r] * rd;
#pragma unroll
        for (int s = 0; s < 2; ++s) {
          u32x4 po = {pk2(o[8 * s], o[8 * s + 1]), pk2(o[8 * s + 2], o[8 * s + 3]), pk2(o[8 * s + 4], o[8 * s + 5]), pk2(o[8 * s + 6], o[8 * s + 7])};
          acc[th][0] = mfma(wf[s][0], __builtin_bit_cast(bf16x8, po), acc[th][0]);
          acc[th][1] = mfma(wf[s][1], __builtin_bit_cast(bf16x8, po), acc[th][1]);
        }
      }
    }
  }
  STAMP(9);
  WAITL0();
  BAR();
  STAMP(10);
#pragma unroll
  for (int th = 0; th < 2; ++th)
#pragma unroll
    for (int cb = 0; cb < 2; ++cb)
#pragma unroll
      for (int q = 0; q < 4; ++q) {
        f32x4 t = {acc[th][cb][4 * q], acc[th][cb][4 * q + 1], acc[th][cb][4 * q + 2], acc[th][cb][4 * q + 3]};
        *(LAS f32x4*)(w * 17408 + (th * 32 + l32) * 272 + (cb * 32 + q * 8 + hh * 4) * 4) = t;
      }
  WAITL0();
  BAR();
  STAMP(11);
  {
    unsigned a = (tid >> 3) * 272 + (tid & 7) * 32;
    f32x4 s0 = ldf4(a), s1 = ldf4(a + 16);
#pragma unroll
    for (int p = 1; p < 8; ++p) { s0 += ldf4(a + p * 17408); s1 += ldf4(a + p * 17408 + 16); }
    float r[8];
#pragma unroll
    for (int i = 0; i < 4; ++i) {
      float lo = __uint_as_float(xin[i] << 16) + __uint_as_float(bin[i] << 16), hi = __uint_as_float(xin[i] & 0xffff0000u) + __uint_as_float(bin[i] & 0xffff0000u);
      r[2 * i] = lo + (i < 2 ? s0[2 * i] : s1[2 * i - 4]);
      r[2 * i + 1] = hi + (i < 2 ? s0[2 * i + 1] : s1[2 * i - 3]);
    }
    u32x4 o = {pk2(r[0], r[1]), pk2(r[2], r[3]), pk2(r[4], r[5]), pk2(r[6], r[7])};
    *(GAS u32x4*)(out + (otok * 448 + ocol) * 2) = o;
  }
  STAMP(6); STAMP_OUT();
}

__global__ void prek(char* ws) { if (threadIdx.x == 0) ((unsigned long long*)(ws + W_DBG))[140000] = __builtin_amdgcn_s_memrealtime(); }
extern "C" void kernel_launch(void* const* d_in, const int* in_sizes, int n_in, void* d_out, int out_size, void* d_ws, size_t ws_size,
                              hipStream_t stream) {
  (void)hipFuncSetAttribute((const void*)fused_kernel, hipFuncAttributeMaxDynamicSharedMemorySize, LDS_BYTES);
  fused_kernel<<<256, 512, LDS_BYTES, stream>>>((const char*)d_in[0], (const char*)d_in[1], (const char*)d_in[2], (const char*)d_in[4],
                                                (const char*)d_in[5], (const char*)d_in[6], (const char*)d_in[7], (char*)d_out, (char*)d_ws);
  prek<<<1,64,0,stream>>>((char*)d_ws);
}
```

```cpp
#include <hip/hip_runtime.h>
#pragma clang diagnostic ignored "-Wint-to-pointer-cast"

typedef unsigned long long u64;
typedef __attribute__((ext_vector_type(8))) __bf16 bf16x8;
typedef __attribute__((ext_vector_type(2))) __bf16 bf16x2;
typedef __attribute__((ext_vector_type(4))) short s16x4;
typedef __attribute__((ext_vector_type(16))) float f32x16;
typedef __attribute__((ext_vector_type(4))) float f32x4;
typedef __attribute__((ext_vector_type(2))) float f32x2;
typedef __attribute__((ext_vector_type(4))) unsigned u32x4;
typedef __attribute__((ext_vector_type(2))) unsigned u32x2;
typedef __amdgpu_buffer_rsrc_t rsrc_t;
#define GAS __attribute__((address_space(1)))
#define LAS __attribute__((address_space(3)))
#define DI __device__ __forceinline__
#define SCHED() __builtin_amdgcn_sched_barrier(0)
#define BAR() __builtin_amdgcn_s_barrier()
#define WAITV(n) asm volatile("s_waitcnt vmcnt(" #n ")" ::: "memory")
#define WAITL0() asm volatile("s_waitcnt lgkmcnt(0)" ::: "memory")

constexpr unsigned STG = 51456;
constexpr unsigned XOFF = 34816;
constexpr unsigned L_CSP = 154368;
constexpr unsigned L_TCH = 156416;
constexpr unsigned L_RS = 156672;
constexpr unsigned LDS_BYTES = 163840;
constexpr unsigned W_GEN = 0;
constexpr unsigned W_F1 = 4096;
constexpr unsigned W_FA = W_F1 + 256 * 128;
constexpr unsigned W_FK = W_FA + 256 * 128;
constexpr unsigned W_FR = W_FK + 32 * 128;
constexpr unsigned W_FC = W_FR + 2048;
constexpr unsigned W_RS = 81920;
constexpr unsigned W_KVF = 98304;
constexpr unsigned W_CS = 131072;
constexpr unsigned W_KVP = 196608;
constexpr unsigned W_PQ = W_KVP + (1u << 20);
constexpr unsigned W_H = W_PQ + (2u << 20);
constexpr unsigned W_END = W_H + 64u * 112u * 1024u;
constexpr unsigned W_DBG = 16u << 20;
constexpr unsigned OOB = 0x40000000u;
constexpr int SPIN_MAX = 400000;

#define STAMP(i) do {} while (0)
#define STAMP_OUT() do {} while (0)
#define CYC(i) do {} while (0)

DI void glds(unsigned ldsaddr, unsigned voff, rsrc_t r, unsigned soff) {
  asm volatile("s_mov_b32 m0, %0\n\ts_nop 0\n\tbuffer_load_dwordx4 %1, %2, %3 offen lds" ::"s"(ldsaddr), "v"(voff), "s"(r), "s"(soff) : "memory");
}
DI void touch(const char* p) {
  asm volatile("s_mov_b32 m0, %0\n\ts_nop 0\n\tglobal_load_lds_dword %1, off" ::"s"(L_TCH), "v"(p) : "memory");
}
DI bf16x8 ld128(unsigned a) { return *(LAS bf16x8*)a; }
DI f32x4 ldf4(unsigned a) { return *(LAS f32x4*)a; }
DI bf16x8 ldtr(unsigned a, unsigned hi) {
  s16x4 lo = __builtin_amdgcn_ds_read_tr16_b64_v4i16((LAS s16x4*)a);
  s16x4 h = __builtin_amdgcn_ds_read_tr16_b64_v4i16((LAS s16x4*)(a + hi));
  return __builtin_bit_cast(bf16x8, __builtin_shufflevector(lo, h, 0, 1, 2, 3, 4, 5, 6, 7));
}
DI f32x16 mfma(bf16x8 a, bf16x8 b, f32x16 c) { return __builtin_amdgcn_mfma_f32_32x32x16_bf16(a, b, c, 0, 0, 0); }
DI f32x16 mfmau(u32x4 a, u32x4 b, f32x16 c) { return mfma(__builtin_bit_cast(bf16x8, a), __builtin_bit_cast(bf16x8, b), c); }
DI unsigned pk2(float a, float b) { f32x2 v = {a, b}; return __builtin_bit_cast(unsigned, __builtin_convertvector(v, bf16x2)); }
DI float flo(unsigned u) { return __uint_as_float(u << 16); }
DI float fhi(unsigned u) { return __uint_as_float(u & 0xffff0000u); }
DI float swapadd32(float a, float b) { auto t = __builtin_amdgcn_permlane32_swap(__float_as_uint(a), __float_as_uint(b), false, false); return __uint_as_float(t[0]) + __uint_as_float(t[1]); }
DI float swapadd16(float a, float b) { auto t = __builtin_amdgcn_permlane16_swap(__float_as_uint(a), __float_as_uint(b), false, false); return __uint_as_float(t[0]) + __uint_as_float(t[1]); }
template <int C> DI float dppadd(float x) { return x + __uint_as_float(__builtin_amdgcn_update_dpp(0u, __float_as_uint(x), C, 0xf, 0xf, true)); }
DI float wavesum(float x) {
  x = dppadd<0xB1>(x); x = dppadd<0x4E>(x); x = dppadd<0x141>(x); x = dppadd<0x140>(x);
  x = swapadd16(x, x); return swapadd32(x, x);
}
DI float bcast_hi(float x) { auto t = __builtin_amdgcn_permlane32_swap(__float_as_uint(x), __float_as_uint(x), false, false); return __uint_as_float(t[1]); }
DI u32x4 ldsc(rsrc_t r, unsigned voff, unsigned soff) { return __builtin_amdgcn_raw_buffer_load_b128(r, voff, soff, 16); }
DI void stsc(u32x4 v, rsrc_t r, unsigned voff, unsigned soff) { __builtin_amdgcn_raw_buffer_store_b128(v, r, voff, soff, 16); }
DI float exp2c(float t) { return __builtin_amdgcn_fmed3f(__builtin_amdgcn_exp2f(t), 0.f, 1.f); }
DI float phi(float v, float s, float sl) { return fmaxf(fmaf(s, v, 1e-6f), 1e-6f) + exp2c(sl * v); }
DI void wait_flag(const char* ws, unsigned off, u64 tag) {
#pragma unroll 1
  for (int s = 0; s < SPIN_MAX; ++s) {
    u64 v = __hip_atomic_load((GAS u64*)(ws + off), __ATOMIC_RELAXED, __HIP_MEMORY_SCOPE_AGENT);
    if (__all(v == tag)) break;
    __builtin_amdgcn_s_sleep(1);
  }
  __builtin_amdgcn_fence(__ATOMIC_ACQUIRE, "wavefront");
}
DI void set_flag(char* ws, unsigned off, u64 tag) { __hip_atomic_store((GAS u64*)(ws + off), tag, __ATOMIC_RELAXED, __HIP_MEMORY_SCOPE_AGENT); }

struct Ld {
  rsrc_t rx, rw, r2, ro, rws;
  unsigned vx, vw, v2, v2b;
  unsigned rs;
  unsigned w;
  const char* vt;
  unsigned vts;
  unsigned vfs;
  unsigned vfp;
  unsigned vrs, vcs;
  char* ws;
};
template <int K> DI void chunk_piece(const Ld& L, unsigned c, unsigned sb) {
  if (K == 0) glds(sb + XOFF + L.w * 2080, L.vx, L.rx, c * 128);
  else if (K == 3) glds(sb + XOFF + L.w * 2080 + 1040, L.vx, L.rx, c * 128 + 7168);
  else { constexpr int j = K < 3 ? K - 1 : K - 2; glds(sb + L.w * 4352 + j * 1088, L.vw, L.rw, (c * 64 + 2 * j) * L.rs); }
}
DI void issue_chunk(const Ld& L, unsigned c, unsigned sb) {
  chunk_piece<0>(L, c, sb); chunk_piece<1>(L, c, sb); chunk_piece<2>(L, c, sb);
  chunk_piece<3>(L, c, sb); chunk_piece<4>(L, c, sb); chunk_piece<5>(L, c, sb);
}
template <int GI, int J> DI void w2_piece(const Ld& L) {
  constexpr int p = GI * 6 + J;
  const unsigned l = (GI % 3) * STG + L.w * 6144 + J * 1024;
  if (p < 28) glds(l, L.v2, L.r2, L.w * (224 * 896) + p * 7168);
  else glds(l, (p & 3) == 3 ? L.v2b : L.v2, L.ro, L.w * (56 * 896) + ((p - 28) >> 2) * (28 * 896) + (p & 3) * 7168);
}
template <int GI> DI void issue_w2(const Ld& L) {
  w2_piece<GI, 0>(L); w2_piece<GI, 1>(L); w2_piece<GI, 2>(L); w2_piece<GI, 3>(L); w2_piece<GI, 4>(L); w2_piece<GI, 5>(L);
}

template <int NB> struct Frag { bf16x8 x; bf16x8 w[NB]; };
template <int NB, bool TR, int SN, int G, int GI>
DI void step(const Frag<NB>& f, Frag<NB>& n, unsigned sn, const unsigned (&wa)[NB], unsigned xa0, unsigned xa1, const Ld& L, unsigned c, unsigned fb,
             f32x16 (&acc)[NB]) {
#pragma unroll
  for (int b = 0; b < NB; ++b) {
    acc[b] = TR ? mfma(f.w[b], f.x, acc[b]) : mfma(f.x, f.w[b], acc[b]);
    SCHED();
    if (SN >= 0) {
      if (b == 0) n.x = ld128(sn + (SN < 2 ? xa0 : xa1) + (SN & 1) * 32);
      if (2 * b < NB) n.w[2 * b] = ldtr(sn + wa[2 * b] + SN * 8704, 2176);
      if (2 * b + 1 < NB) n.w[2 * b + 1] = ldtr(sn + wa[2 * b + 1] + SN * 8704, 2176);
    }
    if (G >= 0 && b >= NB - 2) {
      constexpr int g0 = G < 0 ? 0 : G, gi = GI < 0 ? 0 : GI;
      if (GI >= 0) { if (b == NB - 2) w2_piece<gi, g0>(L); else w2_piece<gi, g0 + 1>(L); }
      else if (c < 7) { if (b == NB - 2) chunk_piece<g0>(L, c, fb); else chunk_piece<g0 + 1>(L, c, fb); }
    }
    SCHED();
  }
}
template <int NB, bool TR, bool GELU, int CO1, int CO2, int CO3>
DI void kloop(f32x16 (&acc)[NB], const unsigned (&wa)[NB], unsigned xa0, unsigned xa1, Ld& L, u32x2 gen, unsigned (&bb)[4], u64& tag, float& r, float& a, float& ia,
              u64 (&sv)[16]) {
#pragma unroll
  for (int b = 0; b < NB; ++b) for (int q = 0; q < 16; ++q) acc[b][q] = 0.f;
  Frag<NB> f0, f1;
  unsigned sb = 2 * STG, fb = STG;
  f0.x = ld128(sb + xa0);
#pragma unroll
  for (int b = 0; b < NB; ++b) f0.w[b] = ldtr(sb + wa[b], 2176);
#pragma unroll 1
  for (unsigned i = 0; i < 5; ++i) {
    const unsigned nb = sb == 2 * STG ? 0 : sb + STG, c = i == 0 ? 7 : i + 2;
    SCHED();
    step<NB, TR, 1, 0, -1>(f0, f1, sb, wa, xa0, xa1, L, c, fb, acc);
    step<NB, TR, 2, 2, -1>(f1, f0, sb, wa, xa0, xa1, L, c, fb, acc);
    step<NB, TR, 3, 4, -1>(f0, f1, sb, wa, xa0, xa1, L, c, fb, acc);
    if (i < 2) { touch(L.vt); L.vt += L.vts; }
    __builtin_amdgcn_s_waitcnt(0xC07F);
    if (i == 0) WAITV(17); else if (i == 1) WAITV(8); else if (i == 2) WAITV(7); else WAITV(6);
    BAR();
    SCHED();
    step<NB, TR, 0, -1, -1>(f1, f0, nb, wa, xa0, xa1, L, c, fb, acc);
    if (i == 1) {
      unsigned ga, gb;
      asm volatile("v_readfirstlane_b32 %0, %2\n\tv_readfirstlane_b32 %1, %3" : "=s"(ga), "=s"(gb) : "v"(gen[0]), "v"(gen[1]));
      const u64 g0 = ((u64)gb << 32) | ga;
      tag = g0 * 6364136223846793005ull + 1442695040888963407ull;
      if (threadIdx.x < 2) set_flag(L.ws, L.vfs, tag);
    }
    fb = sb; sb = nb;
  }
  u32x2 pf, rs; unsigned cq[NB][2];
  SCHED();
  asm volatile("buffer_load_dwordx2 %0, %1, %2, 0 offen sc1" : "=&v"(pf) : "v"(L.vfp), "s"(L.rws) : "memory");
  step<NB, TR, 1, 0, 0>(f0, f1, STG, wa, xa0, xa1, L, 0, 0, acc);
  step<NB, TR, 2, 2, 0>(f1, f0, STG, wa, xa0, xa1, L, 0, 0, acc);
  step<NB, TR, 3, 4, 0>(f0, f1, STG, wa, xa0, xa1, L, 0, 0, acc);
  __builtin_amdgcn_s_waitcnt(0xC07F);
  asm volatile("s_waitcnt vmcnt(6)" : "+v"(pf)::"memory");
  BAR();
  SCHED();
  step<NB, TR, 0, -1, -1>(f1, f0, 2 * STG, wa, xa0, xa1, L, 0, 0, acc);
  if (!__all(pf[0] == (unsigned)tag && pf[1] == (unsigned)(tag >> 32))) wait_flag(L.ws, L.vfp, tag);
  SCHED();
  asm volatile("buffer_load_dwordx2 %0, %1, %2, 0 offen sc1" : "=&v"(rs) : "v"(L.vrs), "s"(L.rws) : "memory");
#pragma unroll
  for (int b = 0; b < NB; ++b) {
    const unsigned co = b == 0 ? 0 : (b == 1 ? CO1 : (b == 2 ? CO2 : CO3));
    asm volatile("buffer_load_dword %0, %2, %3, %4 offen sc1\n\tbuffer_load_dword %1, %2, %3, %5 offen sc1"
                 : "=&v"(cq[b][0]), "=&v"(cq[b][1]) : "v"(L.vcs), "s"(L.rws), "s"(co), "s"(co + 12800) : "memory");
  }
  step<NB, TR, 1, 0, 1>(f0, f1, 2 * STG, wa, xa0, xa1, L, 0, 0, acc);
  step<NB, TR, 2, 2, 1>(f1, f0, 2 * STG, wa, xa0, xa1, L, 0, 0, acc);
  step<NB, TR, 3, 4, 1>(f0, f1, 2 * STG, wa, xa0, xa1, L, 0, 0, acc);
  __builtin_amdgcn_s_waitcnt(0xC07F);
  asm volatile("s_waitcnt vmcnt(6)" : "+v"(rs), "+v"(bb[0]), "+v"(bb[1]), "+v"(bb[2]), "+v"(bb[3])::"memory");
#pragma unroll
  for (int b = 0; b < NB; ++b) asm volatile("" : "+v"(cq[b][0]), "+v"(cq[b][1]));
  BAR();
  SCHED();
  step<NB, TR, -1, -1, -1>(f1, f0, 0, wa, xa0, xa1, L, 0, 0, acc);
  r = __uint_as_float(rs[0]);
  const float nm = -__uint_as_float(rs[1]);
  const unsigned mh = pk2(nm, nm);
  const float ml = nm - flo(mh);
  u32x4 xf = {mh, mh, pk2(ml, ml), 0u};
  a = r;
  if (GELU) {
    a = r * __builtin_amdgcn_rsqf(fmaf(-1e-5f * r, r, 1.f + 1e-5f));
    ia = __builtin_amdgcn_rcpf(a);
    const unsigned p1 = pk2(ia, ia);
    const float l1 = ia - flo(p1);
    const unsigned p2 = pk2(ia, l1);
    xf[3] = (threadIdx.x & 32) ? pk2(l1 - fhi(p2), 0.f) : p2;
  }
#pragma unroll
  for (int b = 0; b < NB; ++b) {
    u32x4 wf = {cq[b][0], cq[b][1], __builtin_amdgcn_perm(cq[b][1], cq[b][0], 0x05040100u), GELU ? bb[b] * 0x10001u : 0u};
    acc[b] = TR ? mfmau(wf, xf, acc[b]) : mfmau(xf, wf, acc[b]);
  }
  SCHED();
}
template <int N0, int N1> DI void w2_to_regs(bf16x8 (&w2r)[9][2], unsigned a20, unsigned a21) {
#pragma unroll
  for (int n = N0; n < N1; ++n) {
    w2r[n][0] = ldtr(a20 + (n / 3) * STG + (n % 3) * 2048, 1024);
    w2r[n][1] = ldtr(a21 + (n / 3) * STG + (n % 3) * 2048, 1024);
  }
  WAITL0();
  SCHED();
}
DI void st_frag(const float (&v)[16], rsrc_t rws, unsigned voff, unsigned soff) {
  u32x4 p0 = {pk2(v[0], v[1]), pk2(v[2], v[3]), pk2(v[4], v[5]), pk2(v[6], v[7])};
  u32x4 p1 = {pk2(v[8], v[9]), pk2(v[10], v[11]), pk2(v[12], v[13]), pk2(v[14], v[15])};
  stsc(p0, rws, voff, soff);
  stsc(p1, rws, voff, soff + 1024);
}
DI void gelu16(const f32x16& acc, float (&v)[16], float k1, float k0, float ia) {
#pragma unroll
  for (int i = 0; i < 16; ++i) {
    const float t = acc[i];
    const float e = __builtin_amdgcn_exp2f(t * fmaf(t * t, k1, k0));
    v[i] = t * __builtin_amdgcn_rcpf(fmaf(e, ia, ia));
  }
}

template <int NB>
DI void unit_w1(Ld& L, unsigned o0, unsigned b0, u32x2 gen, unsigned (&bb)[4], u64& tag, unsigned xa0, unsigned xa1, unsigned wl, unsigned par, unsigned lane,
                unsigned hsoff, bf16x8 (&w2r)[9][2], unsigned a20, unsigned a21, unsigned fidx, unsigned vfa, u64 (&sv)[16]) {
  f32x16 acc[NB]; unsigned wa[NB];
#pragma unroll
  for (int b = 0; b < NB; ++b) wa[b] = wl + ((o0 + (b0 + b) * 64) ^ par);
  float r, a, ia;
  kloop<NB, true, true, 128, 256, 384>(acc, wa, xa0, xa1, L, gen, bb, tag, r, a, ia, sv);
  STAMP(1);
  const float k0 = -2.302208198144325f * a, k1 = -0.10294323970248637f * a * a * a;
  float v[16];
  hsoff += b0 * 2048;
  issue_w2<2>(L);
  w2_to_regs<0, 3>(w2r, a20, a21);
  issue_w2<3>(L);
  STAMP(12);
  gelu16(acc[0], v, k1, k0, ia); st_frag(v, L.rws, lane * 16, hsoff);
  SCHED();
  WAITV(14);
  w2_to_regs<3, 6>(w2r, a20, a21);
  issue_w2<4>(L);
  gelu16(acc[1], v, k1, k0, ia); st_frag(v, L.rws, lane * 16, hsoff + 2048);
  SCHED();
  WAITV(16);
  w2_to_regs<6, 9>(w2r, a20, a21);
  issue_w2<5>(L);
  gelu16(acc[2], v, k1, k0, ia); st_frag(v, L.rws, lane * 16, hsoff + 4096);
  SCHED();
  STAMP(13);
  WAITV(8);
  BAR();
  if (threadIdx.x == 0) set_flag(L.ws, W_FA + fidx, tag);
  SCHED();
  if (NB == 4) {
    gelu16(acc[NB - 1], v, k1, k0, ia);
    SCHED();
    u32x2 pf;
    asm volatile("buffer_load_dwordx2 %0, %1, %2, 0 offen sc1" : "=&v"(pf) : "v"(vfa), "s"(L.rws) : "memory");
    st_frag(v, L.rws, lane * 16, hsoff + 6144);
    STAMP(8);
    asm volatile("s_waitcnt vmcnt(2)" : "+v"(pf)::"memory");
    if (!(pf[0] == (unsigned)tag && pf[1] == (unsigned)(tag >> 32))) wait_flag(L.ws, vfa, tag);
  } else {
    STAMP(8);
    wait_flag(L.ws, vfa, tag);
  }
}
DI void unit_q(Ld& L, unsigned o0, unsigned b0, u32x2 gen, unsigned (&bb)[4], u64& tag, unsigned xa0, unsigned xa1, unsigned wl, unsigned par, unsigned lane,
               unsigned qsoff, u64 (&sv)[16]) {
  f32x16 acc[4]; unsigned wa[4];
#pragma unroll
  for (int b = 0; b < 4; ++b) wa[b] = wl + ((o0 + (b0 + b) * 56) ^ par);
  float r, a, ia;
  kloop<4, true, false, 112, 224, 336>(acc, wa, xa0, xa1, L, gen, bb, tag, r, a, ia, sv);
  STAMP(1);
  const float rl = r * 1.4426950408889634f;
#pragma unroll
  for (int b = 0; b < 4; ++b) {
    float v[16];
#pragma unroll
    for (int i = 0; i < 16; ++i) { const float t = phi(acc[b][i], r, rl); v[i] = (i >= 12 && lane >= 32) ? 0.f : t; }
    st_frag(v, L.rws, lane * 16, qsoff + (b0 + b) * 2048);
    SCHED();
  }
  STAMP(8);
  WAITV(0);
}
template <int NH>
DI void unit_kv(Ld& L, unsigned o0, unsigned h0, u32x2 gen, unsigned (&bb)[4], u64& tag, unsigned xa0, unsigned xa1, unsigned wl, unsigned par, unsigned lane,
                bf16x8 (&w2r)[9][2], unsigned a20, unsigned a21, u64 (&sv)[16]) {
  f32x16 acc[2 * NH]; unsigned wa[2 * NH];
#pragma unroll
  for (int b = 0; b < 2 * NH; ++b) wa[b] = wl + ((o0 + (h0 + (b >> 1)) * 56 + (b & 1) * 256) ^ par);
  float r, a, ia;
  kloop<2 * NH, false, false, 1792, 112, 1904>(acc, wa, xa0, xa1, L, gen, bb, tag, r, a, ia, sv);
  STAMP(1);
  const unsigned l32 = lane & 31, hh = lane >> 5, g = L.w & 3;
  *(LAS float*)(L_RS + (g * 32 + l32) * 4) = r;
  w2_to_regs<0, 3>(w2r, a20, a21);
  issue_w2<3>(L);
  f32x4 ra[4];
#pragma unroll
  for (int q = 0; q < 4; ++q) ra[q] = ldf4(L_RS + (g * 32 + q * 8 + hh * 4) * 4);
  const unsigned vpad = l32 == 28 ? 0x3f803f80u : 0u;
#pragma unroll
  for (int h = 0; h < NH; ++h) {
    f32x16 kv;
#pragma unroll
    for (int i = 0; i < 16; ++i) kv[i] = 0.f;
#pragma unroll
    for (int s = 0; s < 2; ++s) {
      u32x4 pk, pv;
#pragma unroll
      for (int e = 0; e < 4; ++e) {
        const int i = 8 * s + 2 * e;
        const float r0 = ra[i >> 2][i & 3], r1 = ra[i >> 2][(i & 3) + 1];
        const unsigned kk = pk2(phi(acc[2 * h][i], r0, r0 * 1.4426950408889634f), phi(acc[2 * h][i + 1], r1, r1 * 1.4426950408889634f));
        const unsigned vv = pk2(r0 * acc[2 * h + 1][i], r1 * acc[2 * h + 1][i + 1]);
        pk[e] = l32 < 28 ? kk : 0u;
        pv[e] = l32 < 28 ? vv : vpad;
      }
      kv = mfmau(pk, pv, kv);
    }
#pragma unroll
    for (int q = 0; q < 4; ++q) {
      f32x4 t = {kv[4 * q], kv[4 * q + 1], kv[4 * q + 2], kv[4 * q + 3]};
      *(LAS f32x4*)(2 * STG + (((h0 + h) * 4 + g) * 4 + q) * 1024 + lane * 16) = t;
    }
  }
  WAITL0();
}

__global__ __launch_bounds__(512, 2) void fused_kernel(const char* __restrict__ x, const char* __restrict__ wqkv, const char* __restrict__ wo,
                                                       const char* __restrict__ w1, const char* __restrict__ b1, const char* __restrict__ w2,
                                                       const char* __restrict__ b2, char* __restrict__ out, char* ws) {
  extern __shared__ __attribute__((aligned(16))) char lds_dyn[];
  const unsigned tid = threadIdx.x, lane = tid & 63, w = __builtin_amdgcn_readfirstlane(tid >> 6);
  const unsigned xcd = blockIdx.x & 7, bi = blockIdx.x >> 3;
  u64 sv[16] = {0, 0, 0, 0, 0, 0, 0, 0, 0, 0, 0, 0, 0, 0, 0, 0};
  CYC(14);
  STAMP(0);
  const unsigned pg = xcd >> 1, half = xcd & 1, panel = pg * 4 + (bi >> 3), jj = bi & 7;
  const bool is_w1 = jj < 4, is_q = jj == 7;
  const unsigned j = is_w1 ? half * 4 + jj : half * 3 + jj - 4;
  const unsigned slot = is_w1 ? j : (is_q ? 14 + half : 8 + j);
  const unsigned T = xcd * 4 + (bi >> 3), ct = jj;
  const unsigned kvh0 = j < 2 ? 3 * j : (j < 4 ? 2 * j + 2 : 3 * j - 2);
  const bool three = j < 2 || j >= 4;
  const unsigned grp = w >> 2, b0w = grp * 4, h0 = grp ? (three ? 2 : 1) : 0;
  unsigned seg0, seg1, o0, c0;
  if (is_w1) { seg0 = (448 * j) & ~127u; seg1 = seg0 + 256; o0 = (j & 1) * 64; c0 = 224 * j + b0w * 32; }
  else if (is_q) { seg0 = half * 384; seg1 = seg0 + 256; o0 = half * 64; c0 = 1792 + (half * 8 + b0w) * 28; }
  else { seg0 = 896 + 128 * j; seg1 = 1792 + 128 * j; o0 = 56 * kvh0 - 128 * j; c0 = 2240 + (kvh0 + h0) * 28; }
  const char* wsrc = is_w1 ? w1 : wqkv;

  Ld L;
  L.w = w; L.ws = ws;
  L.rs = is_w1 ? 3584 : 2688;
  L.rx = __builtin_amdgcn_make_buffer_rsrc((void*)(x + panel * 128 * 896), 0, 128 * 896, 0x00020000);
  L.rw = __builtin_amdgcn_make_buffer_rsrc((void*)wsrc, 0, 448 * L.rs, 0x00020000);
  const unsigned r8 = lane >> 3, c16 = ((lane & 7) ^ ((lane >> 2) & 4)) * 16;
  const unsigned tk = bi - 14, tq = half ? (tk < 3 ? tk + 4 : (tk < 6 ? tk + 8 : tk + 11)) : (tk >> 2) * 7 + (tk & 3);
  const bool tw1 = bi < 14, ton = bi < 25 - half;
  const unsigned lc = tw1 ? half * 14 + bi : (ton ? 28 + tq : 49), trs = tw1 ? 3584 : 2688;
  u32x4 xr, c0r, c1r;
  {
    const rsrc_t rt = __builtin_amdgcn_make_buffer_rsrc((void*)(tw1 ? w1 : wqkv), 0, 448 * trs, 0x00020000);
    const unsigned vxr = lane < 56 ? (slot * 8 + w) * 896 + lane * 16 : OOB;
    const unsigned vc = (ton && w < 7) ? (pg * 112 + w * 16 + r8) * trs + (tw1 ? lc : tq) * 128 + (lane & 7) * 16 : OOB;
    asm volatile("s_nop 4\n\tbuffer_load_dwordx4 %0, %3, %4, 0 offen\n\tbuffer_load_dwordx4 %1, %5, %6, 0 offen\n\tbuffer_load_dwordx4 %2, %5, %6, %7 offen"
                 : "=&v"(xr), "=&v"(c0r), "=&v"(c1r) : "v"(vxr), "s"(L.rx), "v"(vc), "s"(rt), "s"(8 * trs) : "memory");
  }
  {
    L.vx = (w * 16 + r8) * 896 + c16;
    unsigned rp = lane >> 5, sp = ((lane & 31) * 16) ^ (rp * 128);
    L.vw = (w * 8 + rp) * L.rs + ((sp & 256) ? seg1 : seg0) + (sp & 255);
  }
  issue_chunk(L, 0, 2 * STG);
  issue_chunk(L, 1, 0);
  {
    const bool tx = lane >= 8;
    const unsigned ow = (128 + (panel & 3) * 16 + w * 2 + ((lane >> 2) & 1)) * L.rs + ((lane & 2) ? seg1 : seg0) + (lane & 1) * 128;
    const unsigned ox = (panel * 128 + jj * 16 + w * 2 + (lane & 1)) * 896 + 256;
    L.vt = tx ? x + ox : wsrc + ow;
    L.vts = tx ? 128 : 64 * L.rs;
  }
  touch(L.vt); L.vt += L.vts;
  touch(L.vt); L.vt += L.vts;
  touch(L.vt); L.vt += L.vts;
  u32x2 gen; unsigned bb[4];
  {
    unsigned zero = 0, bo = is_w1 ? (c0 + (lane & 31)) * 2 : 0;
    unsigned bo3 = grp ? bo : bo + 192;
    asm volatile("global_load_dwordx2 %0, %5, %6 sc1\n\tglobal_load_ushort %1, %7, %8\n\tglobal_load_ushort %2, %7, %8 offset:64\n\t"
                 "global_load_ushort %3, %7, %8 offset:128\n\tglobal_load_ushort %4, %9, %8"
                 : "=&v"(gen), "=&v"(bb[0]), "=&v"(bb[1]), "=&v"(bb[2]), "=&v"(bb[3]) : "v"(zero), "s"(ws), "v"(bo), "s"(b1), "v"(bo3) : "memory");
  }
  STAMP(12);
  L.r2 = __builtin_amdgcn_make_buffer_rsrc((void*)w2, 0, 1792 * 896, 0x00020000);
  L.ro = __builtin_amdgcn_make_buffer_rsrc((void*)wo, 0, 448 * 896, 0x00020000);
  L.rws = __builtin_amdgcn_make_buffer_rsrc((void*)ws, 0, W_END, 0x00020000);
  L.v2 = is_q ? OOB : r8 * 896 + c16 + ct * 128;
  L.v2b = (r8 & 3) * 896 + c16 + ct * 128;
  const unsigned l32 = lane & 31, hh = lane >> 5, q4 = (lane & 15) >> 2, blk = (lane >> 4) & 1, p4 = lane & 3;
  L.vfs = lane ? W_FC + (lc * 4 + pg) * 8 : W_FR + (panel * 16 + slot) * 8;
  L.vfp = (lane & 16) ? W_FC + (((is_w1 ? 0 : 28) + (((lane & 8) ? seg1 : seg0) >> 7) + ((lane >> 2) & 1)) * 4 + (lane & 3)) * 8 : W_FR + (panel * 16 + (lane & 15)) * 8;
  L.vrs = W_RS + (panel * 128 + (w & 3) * 32 + l32) * 8;
  L.vcs = W_CS + (hh * 6400 + c0 + l32) * 4;
  unsigned xa0, xa1;
  {
    unsigned r = (w & 3) * 32 + l32, f = (r >> 1) & 1, base = XOFF + (r >> 3) * 1040 + (r & 7) * 128 + hh * 16;
    xa0 = base + f * 64; xa1 = base + (f ^ 1) * 64;
  }
  const unsigned par = (q4 & 1) * 128, wl = 1088 * (4 * hh + (q4 >> 1)) + (q4 & 1) * 512;
  o0 += blk * 32 + p4 * 8;
  const unsigned a20 = (4 * hh + q4) * 128 + (q4 >> 1) * 64 + blk * 32 + p4 * 8 + w * 6144, a21 = a20 ^ 64;
  asm volatile("s_waitcnt vmcnt(20)" : "+v"(xr), "+v"(c0r), "+v"(c1r)::"memory");
  STAMP(13);
  {
    float u[4], e[8];
#pragma unroll
    for (int i = 0; i < 2; ++i) {
      u[2 * i] = swapadd32(flo(c0r[i]) + flo(c1r[i]), flo(c0r[i + 2]) + flo(c1r[i + 2]));
      u[2 * i + 1] = swapadd32(fhi(c0r[i]) + fhi(c1r[i]), fhi(c0r[i + 2]) + fhi(c1r[i + 2]));
    }
    f32x2 t = {dppadd<0x128>(swapadd16(u[0], u[2])), dppadd<0x128>(swapadd16(u[1], u[3]))};
    *(LAS f32x2*)(L_CSP + w * 256 + ((lane & 7) * 8 + hh * 4 + blk * 2) * 4) = t;
#pragma unroll
    for (int i = 0; i < 4; ++i) { e[2 * i] = flo(xr[i]); e[2 * i + 1] = fhi(xr[i]); }
    const float mu = wavesum(((e[0] + e[1]) + (e[2] + e[3])) + ((e[4] + e[5]) + (e[6] + e[7]))) * (1.f / 448.f);
    float s2 = 0.f;
#pragma unroll
    for (int i = 0; i < 8; ++i) { const float d = e[i] - mu; s2 = fmaf(d, d, s2); }
    const float var = wavesum(lane < 56 ? s2 : 0.f) * (1.f / 448.f);
    u32x2 st = {__float_as_uint(__builtin_amdgcn_rsqf(var + 1e-5f)), __float_as_uint(mu)};
    __builtin_amdgcn_raw_buffer_store_b64(st, L.rws, lane ? OOB : W_RS + (panel * 128 + slot * 8 + w) * 8, 0, 16);
    WAITL0();
    BAR();
    const unsigned a = L_CSP + w * 32 + (lane & 1) * 16;
    f32x4 s = ((ldf4(a) + ldf4(a + 256)) + (ldf4(a + 512) + ldf4(a + 768))) + ((ldf4(a + 1024) + ldf4(a + 1280)) + ldf4(a + 1536));
    u32x4 o;
#pragma unroll
    for (int i = 0; i < 4; ++i) o[i] = pk2(s[i], s[i] - flo(pk2(s[i], s[i])));
    stsc(o, L.rws, lane < 2 ? W_CS + (pg * 3200 + lc * 64 + w * 8 + lane * 4) * 4 : OOB, 0);
    SCHED();
  }
  issue_chunk(L, 2, STG);
  WAITV(22);
  BAR();
  STAMP(7);

  bf16x8 w2r[9][2];
  u64 tag = 0;
  const unsigned p2 = T >> 1, fidx = (panel * 16 + slot) * 128, fsrc = (p2 * 16 + w) * 128;
  if (is_w1) {
    unsigned hsoff = W_H + ((panel * 4 + (w & 3)) * 112 + j * 14) * 1024;
    if (grp == 0) unit_w1<4>(L, o0, 0, gen, bb, tag, xa0, xa1, wl, par, lane, hsoff, w2r, a20, a21, fidx, W_FA + fsrc, sv);
    else unit_w1<3>(L, o0, 4, gen, bb, tag, xa0, xa1, wl, par, lane, hsoff, w2r, a20, a21, fidx, W_FA + fsrc, sv);
  } else if (is_q) {
    unsigned qsoff = W_PQ + ((panel * 4 + (w & 3)) * 16 + half * 8) * 2048;
    unit_q(L, o0, b0w, gen, bb, tag, xa0, xa1, wl, par, lane, qsoff, sv);
    BAR();
    if (tid == 0) set_flag(ws, W_F1 + fidx, tag);
    STAMP(2);
    const unsigned rj = panel * 2 + half, head = rj >> 1, sh = rj & 1;
    if (w == 1) {
      const unsigned gj = head < 6 ? head / 3 : (head < 10 ? (head - 2) >> 1 : (head + 2) / 3);
      wait_flag(ws, W_F1 + ((lane & 15) * 16 + 8 + gj) * 128, tag);
      STAMP(3);
      u32x4 t[16][2];
#pragma unroll
      for (int p = 0; p < 16; ++p) {
        t[p][0] = ldsc(L.rws, lane * 16, W_KVP + (p * 16 + head) * 4096 + sh * 2048);
        t[p][1] = ldsc(L.rws, lane * 16, W_KVP + (p * 16 + head) * 4096 + sh * 2048 + 1024);
      }
      f32x4 s0 = __builtin_bit_cast(f32x4, t[0][0]), s1 = __builtin_bit_cast(f32x4, t[0][1]);
#pragma unroll
      for (int p = 1; p < 16; ++p) { s0 += __builtin_bit_cast(f32x4, t[p][0]); s1 += __builtin_bit_cast(f32x4, t[p][1]); }
      u32x4 o = {pk2(s0[0], s0[1]), pk2(s0[2], s0[3]), pk2(s1[0], s1[1]), pk2(s1[2], s1[3])};
      stsc(o, L.rws, lane * 16, W_KVF + rj * 1024);
      WAITV(0);
      if (lane == 0) set_flag(ws, W_FK + rj * 128, tag);
      STAMP(4);
    } else if (w == 2 && rj == 0) {
#pragma unroll 1
      for (int k = 0; k < 4; ++k) wait_flag(ws, W_F1 + (k * 64 + lane) * 128, tag);
      if (lane == 0) set_flag(ws, W_GEN, tag);
    }
    STAMP(6); STAMP_OUT();
    return;
  } else {
    if (grp == 0 && three) unit_kv<2>(L, o0, 0, gen, bb, tag, xa0, xa1, wl, par, lane, w2r, a20, a21, sv);
    else unit_kv<1>(L, o0, h0, gen, bb, tag, xa0, xa1, wl, par, lane, w2r, a20, a21, sv);
    BAR();
    for (unsigned it = w; it < (three ? 12u : 8u); it += 8) {
      unsigned a = 2 * STG + ((it >> 2) * 16 + (it & 3)) * 1024 + lane * 16;
      f32x4 s = (ldf4(a) + ldf4(a + 4096)) + (ldf4(a + 8192) + ldf4(a + 12288));
      stsc(__builtin_bit_cast(u32x4, s), L.rws, lane * 16, W_KVP + (panel * 16 + kvh0) * 4096 + it * 1024);
    }
    SCHED();
    STAMP(8);
    WAITV(7);
    w2_to_regs<3, 6>(w2r, a20, a21);
    issue_w2<4>(L);
    WAITV(6);
    BAR();
    if (tid == 0) set_flag(ws, W_F1 + fidx, tag);
    STAMP(2);
    issue_w2<2>(L);
    WAITV(0);
    w2_to_regs<6, 9>(w2r, a20, a21);
    issue_w2<5>(L);
    wait_flag(ws, W_FA + fsrc, tag);
  }
  STAMP(3);

  f32x16 acc[2][2];
#pragma unroll
  for (int i = 0; i < 4; ++i)
#pragma unroll
    for (int r = 0; r < 16; ++r) acc[i >> 1][i & 1][r] = 0.f;
  const unsigned hb = W_H + (T * 2 * 112 + w * 14) * 1024;
  auto hstep = [&](int n, const u32x4& h0_, const u32x4& h1_) {
    bf16x8 wa, wb;
    if (n < 9) { wa = w2r[n][0]; wb = w2r[n][1]; }
    else { wa = ldtr(a20 + ((n - 9) / 3) * STG + ((n - 9) % 3) * 2048, 1024); wb = ldtr(a21 + ((n - 9) / 3) * STG + ((n - 9) % 3) * 2048, 1024); }
    bf16x8 ha = __builtin_bit_cast(bf16x8, h0_), hb_ = __builtin_bit_cast(bf16x8, h1_);
    acc[0][0] = mfma(wa, ha, acc[0][0]);
    acc[0][1] = mfma(wb, ha, acc[0][1]);
    acc[1][0] = mfma(wa, hb_, acc[1][0]);
    acc[1][1] = mfma(wb, hb_, acc[1][1]);
  };
  {
    u32x4 h[8][2];
#pragma unroll
    for (int n = 0; n < 8; ++n) { const int k = n < 4 ? n : n + 4; h[n][0] = ldsc(L.rws, lane * 16, hb + k * 1024); h[n][1] = ldsc(L.rws, lane * 16, hb + (112 + k) * 1024); }
    SCHED();
    if (is_w1) {
      WAITV(16);
      BAR();
      if (tid == 0) set_flag(ws, W_F1 + fidx, tag);
      STAMP(2);
    }
    SCHED();
#pragma unroll
    for (int n = 0; n < 8; ++n) { hstep(n < 4 ? n : n + 4, h[n][0], h[n][1]); SCHED(); }
  }
  STAMP(4);
  const unsigned otok = T * 64 + (tid >> 3), ocol = ct * 64 + (tid & 7) * 8;
  u32x4 pq[2][2][2], kf[2][2];
  {
    wait_flag(ws, W_F1 + (lane ? fsrc : (p2 * 16 + 14 + (w >> 2)) * 128), tag);
    STAMP(5);
    u32x4 h[6][2];
#pragma unroll
    for (int n = 0; n < 6; ++n) { const int k = n < 4 ? n + 4 : n + 8; h[n][0] = ldsc(L.rws, lane * 16, hb + k * 1024); h[n][1] = ldsc(L.rws, lane * 16, hb + (112 + k) * 1024); }
#pragma unroll
    for (int th = 0; th < 2; ++th)
#pragma unroll
      for (int hd = 0; hd < 2; ++hd)
#pragma unroll
        for (int s = 0; s < 2; ++s) pq[hd][th][s] = ldsc(L.rws, lane * 16, W_PQ + (((T * 2 + th) * 16 + w * 2 + hd) * 2 + s) * 1024);
    SCHED();
#pragma unroll
    for (int n = 0; n < 6; ++n) { hstep(n < 4 ? n + 4 : n + 8, h[n][0], h[n][1]); SCHED(); }
  }
  const u32x4 xin = *(const GAS u32x4*)(x + (otok * 448 + ocol) * 2);
  const u32x4 bin = *(const GAS u32x4*)(b2 + ocol * 2);
  STAMP(9);
  wait_flag(ws, W_FK + (w * 4 + (lane & 3)) * 128, tag);
  STAMP(10);
#pragma unroll
  for (int hd = 0; hd < 2; ++hd)
#pragma unroll
    for (int s = 0; s < 2; ++s) kf[hd][s] = ldsc(L.rws, lane * 16, W_KVF + ((w * 2 + hd) * 2 + s) * 1024);
  bf16x8 wf[2][2][2];
#pragma unroll
  for (int hd = 0; hd < 2; ++hd)
#pragma unroll
    for (int s = 0; s < 2; ++s) {
      const int n = 5 + hd * 2 + s;
      wf[hd][s][0] = ldtr(a20 + (n / 3) * STG + (n % 3) * 2048, 1024);
      wf[hd][s][1] = ldtr(a21 + (n / 3) * STG + (n % 3) * 2048, 1024);
    }
  WAITL0();
  SCHED();
#pragma unroll
  for (int th = 0; th < 2; ++th) {
#pragma unroll
    for (int hd = 0; hd < 2; ++hd) {
      f32x16 nm;
#pragma unroll
      for (int r = 0; r < 16; ++r) nm[r] = 0.f;
      nm = mfmau(kf[hd][0], pq[hd][th][0], nm);
      nm = mfmau(kf[hd][1], pq[hd][th][1], nm);
      const float rd = __builtin_amdgcn_rcpf(bcast_hi(nm[12]) + 1e-6f);
      u32x4 po0 = {pk2(nm[0] * rd, nm[1] * rd), pk2(nm[2] * rd, nm[3] * rd), pk2(nm[4] * rd, nm[5] * rd), pk2(nm[6] * rd, nm[7] * rd)};
      u32x4 po1 = {pk2(nm[8] * rd, nm[9] * rd), pk2(nm[10] * rd, nm[11] * rd), hh ? 0u : pk2(nm[12] * rd, nm[13] * rd), hh ? 0u : pk2(nm[14] * rd, nm[15] * rd)};
      acc[th][0] = mfma(wf[hd][0][0], __builtin_bit_cast(bf16x8, po0), acc[th][0]);
      acc[th][1] = mfma(wf[hd][0][1], __builtin_bit_cast(bf16x8, po0), acc[th][1]);
      acc[th][0] = mfma(wf[hd][1][0], __builtin_bit_cast(bf16x8, po1), acc[th][0]);
      acc[th][1] = mfma(wf[hd][1][1], __builtin_bit_cast(bf16x8, po1), acc[th][1]);
    }
    const unsigned row = th * 32 + l32, sl = row >= 44 ? 2 : (row >= 22 ? 1 : 0);
    const unsigned ra = sl * (STG - 22 * 272) + w * 6144 + row * 272 + hh * 128;
#pragma unroll
    for (int cb = 0; cb < 2; ++cb)
#pragma unroll
      for (int q = 0; q < 4; ++q) {
        f32x4 t = {acc[th][cb][4 * q], acc[th][cb][4 * q + 1], acc[th][cb][4 * q + 2], acc[th][cb][4 * q + 3]};
        *(LAS f32x4*)(ra + (cb * 4 + q) * 16) = t;
      }
  }
  WAITL0();
  BAR();
  STAMP(11);
  {
    const unsigned row = tid >> 3, sl = row >= 44 ? 2 : (row >= 22 ? 1 : 0);
    const unsigned a = sl * (STG - 22 * 272) + row * 272 + (tid & 7) * 16;
    f32x4 s0 = ldf4(a), s1 = ldf4(a + 128);
#pragma unroll
    for (int p = 1; p < 8; ++p) { s0 += ldf4(a + p * 6144); s1 += ldf4(a + p * 6144 + 128); }
    float r[8];
#pragma unroll
    for (int i = 0; i < 4; ++i) {
      float lo = flo(xin[i]) + flo(bin[i]), hi = fhi(xin[i]) + fhi(bin[i]);
      r[2 * i] = lo + (i < 2 ? s0[2 * i] : s1[2 * i - 4]);
      r[2 * i + 1] = hi + (i < 2 ? s0[2 * i + 1] : s1[2 * i - 3]);
    }
    u32x4 o = {pk2(r[0], r[1]), pk2(r[2], r[3]), pk2(r[4], r[5]), pk2(r[6], r[7])};
    *(GAS u32x4*)(out + (otok * 448 + ocol) * 2) = o;
  }
  STAMP(6); STAMP_OUT();
}

extern "C" void kernel_launch(void* const* d_in, const int* in_sizes, int n_in, void* d_out, int out_size, void* d_ws, size_t ws_size,
                              hipStream_t stream) {
  (void)hipFuncSetAttribute((const void*)fused_kernel, hipFuncAttributeMaxDynamicSharedMemorySize, LDS_BYTES);
  fused_kernel<<<256, 512, LDS_BYTES, stream>>>((const char*)d_in[0], (const char*)d_in[1], (const char*)d_in[2], (const char*)d_in[4],
                                                (const char*)d_in[5], (const char*)d_in[6], (const char*)d_in[7], (char*)d_out, (char*)d_ws);
}
```

```cpp
#include <hip/hip_runtime.h>
#pragma clang diagnostic ignored "-Wint-to-pointer-cast"

typedef unsigned long long u64;
typedef __attribute__((ext_vector_type(8))) __bf16 bf16x8;
typedef __attribute__((ext_vector_type(2))) __bf16 bf16x2;
typedef __attribute__((ext_vector_type(4))) short s16x4;
typedef __attribute__((ext_vector_type(16))) float f32x16;
typedef __attribute__((ext_vector_type(4))) float f32x4;
typedef __attribute__((ext_vector_type(2))) float f32x2;
typedef __attribute__((ext_vector_type(4))) unsigned u32x4;
typedef __attribute__((ext_vector_type(2))) unsigned u32x2;
typedef __attribute__((ext_vector_type(4))) unsigned long long u64x4;
typedef __amdgpu_buffer_rsrc_t rsrc_t;
#define GAS __attribute__((address_space(1)))
#define LAS __attribute__((address_space(3)))
#define DI __device__ __forceinline__
#define SCHED() __builtin_amdgcn_sched_barrier(0)
#define BAR() __builtin_amdgcn_s_barrier()
#define WAITV(n) asm volatile("s_waitcnt vmcnt(" #n ")" ::: "memory")
#define WAITL0() asm volatile("s_waitcnt lgkmcnt(0)" ::: "memory")

constexpr unsigned STG = 51456;
constexpr unsigned XOFF = 34816;
constexpr unsigned L_CSP = 154368;
constexpr unsigned L_TCH = 156416;
constexpr unsigned L_RS = 156672;
constexpr unsigned L_CQ = 157184;
constexpr unsigned LDS_BYTES = 163840;
constexpr unsigned W_GEN = 0;
constexpr unsigned W_RS = 81920;
constexpr unsigned W_KVF = 4096;
constexpr unsigned W_CS = 147456;
constexpr unsigned W_KVP = 262144;
constexpr unsigned W_PQ = W_KVP + (1u << 20);
constexpr unsigned W_H = W_PQ + (2u << 20);
constexpr unsigned W_FL = W_H + 64u * 112u * 1024u;
constexpr unsigned W_END = W_FL + 2048u * 128u;
constexpr unsigned OOB = 0x40000000u;
constexpr unsigned CODE_BYTES = 58816;
constexpr int SPIN_MAX = 400000;


template <int N> DI void waitv() { asm volatile("s_waitcnt vmcnt(%0)" ::"n"(N) : "memory"); }
template <unsigned LI, unsigned SO> DI void glds(unsigned lb, unsigned voff, rsrc_t r) {
  unsigned t;
  asm volatile("s_add_u32 m0, %1, %2\n\ts_mov_b32 %0, %3\n\tbuffer_load_dwordx4 %4, %5, %0 offen lds"
               : "=&s"(t) : "s"(lb), "n"(LI), "n"(SO), "v"(voff), "s"(r) : "memory", "scc");
}
DI void glds_sc(unsigned lb, unsigned voff, rsrc_t r) {
  asm volatile("s_mov_b32 m0, %0\n\ts_nop 0\n\tbuffer_load_dwordx4 %1, %2, 0 offen sc1 lds" ::"s"(lb), "v"(voff), "s"(r) : "memory");
}
template <unsigned SO> DI void touch(unsigned voff, rsrc_t r) {
  unsigned t;
  asm volatile("s_mov_b32 m0, %1\n\ts_mov_b32 %0, %2\n\tbuffer_load_dword %3, %4, %0 offen sc1 lds" : "=&s"(t) : "n"(L_TCH), "n"(SO), "v"(voff), "s"(r) : "memory");
}
DI bf16x8 ld128(unsigned a) { return *(LAS bf16x8*)a; }
DI f32x4 ldf4(unsigned a) { return *(LAS f32x4*)a; }
DI bf16x8 ldtr(unsigned a, unsigned hi) {
  s16x4 lo = __builtin_amdgcn_ds_read_tr16_b64_v4i16((LAS s16x4*)a);
  s16x4 h = __builtin_amdgcn_ds_read_tr16_b64_v4i16((LAS s16x4*)(a + hi));
  return __builtin_bit_cast(bf16x8, __builtin_shufflevector(lo, h, 0, 1, 2, 3, 4, 5, 6, 7));
}
DI f32x16 mfma(bf16x8 a, bf16x8 b, f32x16 c) { return __builtin_amdgcn_mfma_f32_32x32x16_bf16(a, b, c, 0, 0, 0); }
DI f32x16 mfmau(u32x4 a, u32x4 b, f32x16 c) { return mfma(__builtin_bit_cast(bf16x8, a), __builtin_bit_cast(bf16x8, b), c); }
DI unsigned pk2(float a, float b) { f32x2 v = {a, b}; return __builtin_bit_cast(unsigned, __builtin_convertvector(v, bf16x2)); }
DI float flo(unsigned u) { return __uint_as_float(u << 16); }
DI float fhi(unsigned u) { return __uint_as_float(u & 0xffff0000u); }
DI float swapadd32(float a, float b) { auto t = __builtin_amdgcn_permlane32_swap(__float_as_uint(a), __float_as_uint(b), false, false); return __uint_as_float(t[0]) + __uint_as_float(t[1]); }
DI float swapadd16(float a, float b) { auto t = __builtin_amdgcn_permlane16_swap(__float_as_uint(a), __float_as_uint(b), false, false); return __uint_as_float(t[0]) + __uint_as_float(t[1]); }
template <int C> DI float dppadd(float x) { return x + __uint_as_float(__builtin_amdgcn_update_dpp(0u, __float_as_uint(x), C, 0xf, 0xf, true)); }
DI float halfsum(float x) {
  x = dppadd<0xB1>(x); x = dppadd<0x4E>(x); x = dppadd<0x141>(x); x = dppadd<0x140>(x);
  return swapadd16(x, x);
}
DI unsigned neq2(unsigned a, unsigned b, unsigned t) { return __builtin_amdgcn_bitop3_b32(a, b, t, 0x7e); }
DI float bcast_hi(float x) { auto t = __builtin_amdgcn_permlane32_swap(__float_as_uint(x), __float_as_uint(x), false, false); return __uint_as_float(t[1]); }
DI u32x4 ldsc(rsrc_t r, unsigned voff, unsigned soff) { return __builtin_amdgcn_raw_buffer_load_b128(r, voff, soff, 16); }
DI void stsc0(u32x4 v, rsrc_t r, unsigned voff, unsigned soff) { __builtin_amdgcn_raw_buffer_store_b128(v, r, voff, soff, 16); SCHED(); }
DI void stsc(u32x4 v, rsrc_t r, unsigned voff, unsigned soff) { stsc0(v, r, voff, soff); asm volatile("s_nop 1"); SCHED(); }
DI float exp2c(float t) { return __builtin_amdgcn_fmed3f(__builtin_amdgcn_exp2f(t), 0.f, 1.f); }
DI float phi(float v, float s, float sl) { return fmaxf(fmaf(s, v, 1e-6f), 1e-6f) + exp2c(sl * v); }
DI void wait_flag(const char* ws, unsigned off, u64 tag) {
#pragma unroll 1
  for (int s = 0; s < SPIN_MAX; ++s) {
    u64 v = __hip_atomic_load((GAS u64*)(ws + off), __ATOMIC_RELAXED, __HIP_MEMORY_SCOPE_AGENT);
    if (__all(v == tag)) break;
    __builtin_amdgcn_s_sleep(1);
  }
  __builtin_amdgcn_fence(__ATOMIC_ACQUIRE, "wavefront");
}
DI void set_flag(char* ws, unsigned off, u64 tag) { __hip_atomic_store((GAS u64*)(ws + off), tag, __ATOMIC_RELAXED, __HIP_MEMORY_SCOPE_AGENT); }

DI void wave_flag(rsrc_t r, unsigned voff, unsigned part, u64 tag) {
  u32x2 tv = {(unsigned)tag, (unsigned)(tag >> 32)};
  __builtin_amdgcn_raw_buffer_store_b64(tv, r, voff + part * 8, 0, 16);
  SCHED();
}

struct Ld {
  rsrc_t rx, rw, r2, ro, rws, rout;
  unsigned vx, vw, v2, vo, vob;
  unsigned lw, lx, l2;
  unsigned w;
  unsigned vg2, vgo;
  unsigned vrs, vcs;
  unsigned vsl, lsl;
  unsigned acq;
  unsigned vsr, vsc;
  u32x4 xr[2], cr[4];
  u32x2 gen; unsigned bb[4];
  char* ws;
};
template <unsigned RS, int C, int K> DI void chunk_piece(const Ld& L) {
  constexpr unsigned sb = ((C + 2) % 3) * STG;
  if (K == 0) glds<sb + XOFF, C * 128>(L.lx, L.vx, L.rx);
  else if (K == 3) glds<sb + XOFF + 1040, C * 128 + 7168>(L.lx, L.vx, L.rx);
  else { constexpr int j = K < 3 ? (K > 0 ? K - 1 : 0) : K - 2; glds<sb + j * 1088, (C * 64 + 2 * j) * RS>(L.lw, L.vw, L.rw); }
}
template <unsigned RS, int C> DI void issue_chunk(const Ld& L) {
  chunk_piece<RS, C, 0>(L); chunk_piece<RS, C, 1>(L); chunk_piece<RS, C, 2>(L); chunk_piece<RS, C, 3>(L); chunk_piece<RS, C, 4>(L); chunk_piece<RS, C, 5>(L);
}
constexpr int slot_step(int n) { constexpr int t[18] = {0, 1, 2, 3, 8, 9, 10, 11, 4, 14, 15, 16, 17, 5, 6, 7, 12, 13}; return t[n]; }
template <int GI, int J, int RG = GI % 3> DI void w2_piece(const Ld& L) {
  constexpr int p = slot_step(GI * 3 + J / 2) * 2 + (J & 1), po = p < 28 ? 0 : p - 28;
  constexpr unsigned l = RG * STG + J * 1024;
  if (p < 28) glds<l, p * 7168>(L.l2, L.v2, L.r2);
  else glds<l, (po >> 2) * (28 * 896) + (po & 3) * 7168>(L.l2, (po & 3) == 3 ? L.vob : L.vo, L.ro);
}
template <int GI, int RG = GI % 3> DI void issue_w2(const Ld& L) {
  w2_piece<GI, 0, RG>(L); w2_piece<GI, 1, RG>(L); w2_piece<GI, 2, RG>(L); w2_piece<GI, 3, RG>(L); w2_piece<GI, 4, RG>(L); w2_piece<GI, 5, RG>(L);
}
DI void stats1(const Ld& L, unsigned tag) {
  const unsigned lane = threadIdx.x & 63;
  float u[4], e[16];
#pragma unroll
  for (int i = 0; i < 2; ++i) {
    u[2 * i] = swapadd32((flo(L.cr[0][i]) + flo(L.cr[1][i])) + (flo(L.cr[2][i]) + flo(L.cr[3][i])),
                         (flo(L.cr[0][i + 2]) + flo(L.cr[1][i + 2])) + (flo(L.cr[2][i + 2]) + flo(L.cr[3][i + 2])));
    u[2 * i + 1] = swapadd32((fhi(L.cr[0][i]) + fhi(L.cr[1][i])) + (fhi(L.cr[2][i]) + fhi(L.cr[3][i])),
                             (fhi(L.cr[0][i + 2]) + fhi(L.cr[1][i + 2])) + (fhi(L.cr[2][i + 2]) + fhi(L.cr[3][i + 2])));
  }
  f32x2 c = {dppadd<0x128>(swapadd16(u[0], u[2])), dppadd<0x128>(swapadd16(u[1], u[3]))};
  *(LAS f32x2*)(L_CSP + L.w * 256 + ((lane & 7) * 8 + (lane >> 5) * 4 + ((lane >> 4) & 1) * 2) * 4) = c;
#pragma unroll
  for (int i = 0; i < 8; ++i) { e[2 * i] = flo(L.xr[i >> 2][i & 3]); e[2 * i + 1] = fhi(L.xr[i >> 2][i & 3]); }
  const float mu = halfsum((((e[0] + e[1]) + (e[2] + e[3])) + ((e[4] + e[5]) + (e[6] + e[7]))) +
                           (((e[8] + e[9]) + (e[10] + e[11])) + ((e[12] + e[13]) + (e[14] + e[15])))) * (1.f / 448.f);
  float s2 = 0.f, s3 = 0.f;
#pragma unroll
  for (int i = 0; i < 8; ++i) { const float d = e[i] - mu, g = e[i + 8] - mu; s2 = fmaf(d, d, s2); s3 = fmaf(g, g, s3); }
  const float var = halfsum((lane & 31) < 28 ? s2 + s3 : 0.f);
  const unsigned r = __float_as_uint(__builtin_amdgcn_rsqf(fmaf(var, 1.f / 448.f, 1e-5f)));
  u32x2 st = {(lane & 1) ? __float_as_uint(mu) : r, tag};
  __builtin_amdgcn_raw_buffer_store_b64(st, L.rws, L.vsr, 0, 16);
}
DI float ldf(unsigned a) { return *(LAS float*)a; }
DI void stats2(const Ld& L, unsigned tag) {
  const unsigned a = L_CSP + L.w * 32 + (threadIdx.x & 7) * 4;
  const float s = ((ldf(a) + ldf(a + 256)) + (ldf(a + 512) + ldf(a + 768))) + ((ldf(a + 1024) + ldf(a + 1280)) + ldf(a + 1536));
  u32x2 o = {pk2(s, s - flo(pk2(s, s))), tag};
  __builtin_amdgcn_raw_buffer_store_b64(o, L.rws, L.vsc, 0, 16);
}

template <int NB> struct Frag { bf16x8 x; bf16x8 w[NB]; };
template <int NB, bool TR, unsigned RS, int SN, int C, int G, class F>
DI void step(const Frag<NB>& f, Frag<NB>& n, const unsigned (&wa)[NB], unsigned xa0, unsigned xa1, const Ld& L, f32x16 (&acc)[NB], F fill) {
#pragma unroll
  for (int b = 0; b < NB; ++b) {
    acc[b] = TR ? mfma(f.w[b], f.x, acc[b]) : mfma(f.x, f.w[b], acc[b]);
    asm volatile("" : "+v"(acc[b]));
    SCHED();
    if (SN >= 0) {
      if (b == 0) n.x = ld128((SN < 2 ? xa0 : xa1) + (SN & 1) * 32);
      if (2 * b < NB) n.w[2 * b] = ldtr(wa[2 * b] + SN * 8704, 2176);
      if (2 * b + 1 < NB) n.w[2 * b + 1] = ldtr(wa[2 * b + 1] + SN * 8704, 2176);
    }
    if (b < 2) fill(2 * SN - 2 + b);
    if (G >= 0 && b >= NB - 2) {
      constexpr int g0 = G < 0 ? 0 : G, gi = C < 7 ? 0 : C - 7, c = C < 7 ? C : 0;
      if (C >= 7) { if (b == NB - 2) w2_piece<gi, g0>(L); else w2_piece<gi, g0 + 1>(L); }
      else { if (b == NB - 2) chunk_piece<RS, c, g0>(L); else chunk_piece<RS, c, g0 + 1>(L); }
    }
    SCHED();
  }
}
template <int NB, bool TR, unsigned RS, bool P2, int I, class F>
DI void steps3(Frag<NB>& f0, Frag<NB>& f1, const unsigned (&wa)[NB], unsigned xa0, unsigned xa1, const Ld& L, f32x16 (&acc)[NB], F fill) {
  constexpr bool ld = I < 5 || P2;
  step<NB, TR, RS, 1, I + 2, ld ? 0 : -1>(f0, f1, wa, xa0, xa1, L, acc, fill);
  step<NB, TR, RS, 2, I + 2, ld ? 2 : -1>(f1, f0, wa, xa0, xa1, L, acc, fill);
  step<NB, TR, RS, 3, I + 2, ld ? 4 : -1>(f0, f1, wa, xa0, xa1, L, acc, fill);
}
template <int NB, bool TR, unsigned RS, int I, int WV>
DI void turn(Frag<NB>& f0, Frag<NB>& f1, unsigned (&wa)[NB], unsigned& xa0, unsigned& xa1, const Ld& L, f32x16 (&acc)[NB]) {
  if (I < 6) {
    constexpr unsigned d = I % 3 == 0 ? 0u - 2 * STG : STG;
#pragma unroll
    for (int b = 0; b < NB; ++b) { wa[b] += d; asm volatile("" : "+v"(wa[b])); }
    xa0 += d; xa1 += d;
    asm volatile("" : "+v"(xa0), "+v"(xa1));
  }
  __builtin_amdgcn_s_waitcnt(0xC07F);
  if (WV >= 0) waitv<WV < 0 ? 0 : WV>();
  BAR();
  SCHED();
  step<NB, TR, RS, I < 6 ? 0 : -1, 0, -1>(f1, f0, wa, xa0, xa1, L, acc, [](int) {});
}
#define PRIO(n) do { SCHED(); __builtin_amdgcn_s_setprio(n); SCHED(); } while (0)
constexpr int GT = 1;
constexpr int IL = 1;
DI u64 make_tag(const Ld& L) {
  unsigned ga, gb;
  asm volatile("v_readfirstlane_b32 %0, %2\n\tv_readfirstlane_b32 %1, %3" : "=s"(ga), "=s"(gb) : "v"(L.gen[0]), "v"(L.gen[1]));
  return (((u64)gb << 32) | ga) * 6364136223846793005ull + 1442695040888963407ull;
}
template <int NB, bool TR, bool GELU, bool P2, unsigned RS, int CO1, int CO2, int CO3, class PRO>
DI void kloop(f32x16 (&acc)[NB], unsigned (&wa)[NB], unsigned xa0, unsigned xa1, Ld& L, PRO rest, u64& tag, float& r, float& a, float& ia) {
  if (!GELU) rest();
#pragma unroll
  for (int b = 0; b < NB; ++b) { for (int q = 0; q < 16; ++q) acc[b][q] = 0.f; asm volatile("" : "+v"(acc[b]), "+v"(wa[b])); }
  asm volatile("" : "+v"(xa0), "+v"(xa1));
  SCHED();
  issue_chunk<RS, 1>(L);
  if (GELU) rest();
  if (GT) { touch<0>(L.vg2, L.r2); touch<0>(L.vgo, L.ro); }
  if (GELU) waitv<11 + 2 * GT>();
  else {
    waitv<6 + 2 * GT>();
    SCHED();
    asm volatile("" : "+v"(L.xr[0]), "+v"(L.xr[1]), "+v"(L.cr[0]), "+v"(L.cr[1]), "+v"(L.cr[2]), "+v"(L.cr[3]));
    tag = make_tag(L);
    stats1(L, (unsigned)tag);
    __builtin_amdgcn_s_waitcnt(0xC07F);
    SCHED();
  }
  BAR();
  auto none = [](int) {};
  Frag<NB> f0, f1;
  f0.x = ld128(xa0);
#pragma unroll
  for (int b = 0; b < NB; ++b) f0.w[b] = ldtr(wa[b], 2176);
  SCHED();
  steps3<NB, TR, RS, P2, 0>(f0, f1, wa, xa0, xa1, L, acc, [&](int p) { if (!GELU && p == 0) stats2(L, (unsigned)tag); });
  turn<NB, TR, RS, 0, (GELU ? 11 : 8) + 2 * GT>(f0, f1, wa, xa0, xa1, L, acc);
  steps3<NB, TR, RS, P2, 1>(f0, f1, wa, xa0, xa1, L, acc, none);
  turn<NB, TR, RS, 1, 6>(f0, f1, wa, xa0, xa1, L, acc);
  if (GELU) {
    asm volatile("" : "+v"(L.bb[0]), "+v"(L.bb[1]), "+v"(L.bb[2]), "+v"(L.bb[3]));
    tag = make_tag(L);
  }
  const unsigned tg = (unsigned)tag;
  steps3<NB, TR, RS, P2, 2>(f0, f1, wa, xa0, xa1, L, acc, none);
  turn<NB, TR, RS, 2, 6>(f0, f1, wa, xa0, xa1, L, acc);
  steps3<NB, TR, RS, P2, 3>(f0, f1, wa, xa0, xa1, L, acc, none);
  turn<NB, TR, RS, 3, 6>(f0, f1, wa, xa0, xa1, L, acc);
  steps3<NB, TR, RS, P2, 4>(f0, f1, wa, xa0, xa1, L, acc, none);
  glds_sc(L.lsl, L.vsl, L.rws);
  turn<NB, TR, RS, 4, 7>(f0, f1, wa, xa0, xa1, L, acc);
  steps3<NB, TR, RS, P2, 5>(f0, f1, wa, xa0, xa1, L, acc, none);
  turn<NB, TR, RS, 5, P2 ? 6 : 0>(f0, f1, wa, xa0, xa1, L, acc);
  u32x4 rs, xf; u32x2 cq[NB];
  steps3<NB, TR, RS, P2, 6>(f0, f1, wa, xa0, xa1, L, acc, [&](int p) {
    if (p == 0) {
      rs = *(LAS u32x4*)(L_CQ + 4096 + ((L.w & 3) * 32 + (threadIdx.x & 31)) * 16);
#pragma unroll
      for (int b = 0; b < NB; ++b) {
        const int co = b == 0 ? 0 : (b == 1 ? CO1 : (b == 2 ? CO2 : CO3));
        cq[b] = *(LAS u32x2*)(L.acq + (co >= 448 ? co - 320 : co) * 16);
      }
    } else if (p == 2) {
#pragma unroll 1
      for (int s = 0; s < SPIN_MAX; ++s) {
        unsigned m = (rs[1] ^ tg) | (rs[3] ^ tg);
#pragma unroll
        for (int b = 0; b < NB; ++b) m |= cq[b][1] ^ tg;
        asm volatile("" : "+v"(m));
        if (__all(m == 0)) break;
        asm volatile("buffer_load_dwordx4 %0, %1, %2, 0 offen sc1\n\ts_waitcnt vmcnt(0)" : "=&v"(rs) : "v"(L.vrs), "s"(L.rws) : "memory");
#pragma unroll
        for (int b = 0; b < NB; ++b)
          asm volatile("buffer_load_dwordx2 %0, %1, %2, %3 offen sc1\n\ts_waitcnt vmcnt(0)"
                       : "=&v"(cq[b]) : "v"(L.vcs), "s"(L.rws), "s"((b == 0 ? 0 : (b == 1 ? CO1 : (b == 2 ? CO2 : CO3))) * 16) : "memory");
      }
    } else if (p == 4) {
      r = __uint_as_float(rs[0]);
      const float nm = -__uint_as_float(rs[2]);
      const unsigned mh = pk2(nm, nm);
      const float ml = nm - flo(mh);
      xf = u32x4{mh, pk2(ml, ml), 0u, 0u};
      a = r;
      if (GELU) {
        a = r * __builtin_amdgcn_rsqf(fmaf(-1e-5f * r, r, 1.f + 1e-5f));
        ia = __builtin_amdgcn_rcpf(a);
        const unsigned p1 = pk2(ia, ia);
        const float l1 = ia - flo(p1);
        const unsigned p2 = pk2(ia, l1);
        xf[3] = (threadIdx.x & 32) ? pk2(l1 - fhi(p2), 0.f) : p2;
        asm volatile("" : "+v"(a), "+v"(ia));
      }
      asm volatile("" : "+v"(xf));
    }
  });
#pragma unroll
  for (int b = 0; b < NB; ++b) {
    u32x4 wf = {cq[b][0], cq[b][0], cq[b][0], GELU ? L.bb[b] * 0x10001u : cq[b][0]};
    acc[b] = TR ? mfmau(wf, xf, acc[b]) : mfmau(xf, wf, acc[b]);
    asm volatile("" : "+v"(acc[b]));
  }
  SCHED();
  turn<NB, TR, RS, 6, -1>(f0, f1, wa, xa0, xa1, L, acc);
}
template <int N0, int N1, int RM = 3> DI void w2_to_regs(bf16x8 (&w2r)[9][2], unsigned a20, unsigned a21) {
#pragma unroll
  for (int n = N0; n < N1; ++n) {
    w2r[n][0] = ldtr(a20 + (n / 3 % RM) * STG + (n % 3) * 2048, 1024);
    w2r[n][1] = ldtr(a21 + (n / 3 % RM) * STG + (n % 3) * 2048, 1024);
  }
  SCHED();
}
#define LGKM0() do { __builtin_amdgcn_s_waitcnt(0xC07F); SCHED(); } while (0)
DI u32x2 gelu4(const f32x16& acc, int q, float k1, float k0, float ia) {
  f32x2 v[2];
#pragma unroll
  for (int i = 0; i < 2; ++i) {
    const f32x2 t = {acc[4 * q + 2 * i], acc[4 * q + 2 * i + 1]};
    const f32x2 u = t * __builtin_elementwise_fma(t * t, f32x2{k1, k1}, f32x2{k0, k0});
    const f32x2 e = {__builtin_amdgcn_exp2f(u[0]), __builtin_amdgcn_exp2f(u[1])};
    const f32x2 d = __builtin_elementwise_fma(e, f32x2{ia, ia}, f32x2{ia, ia});
    v[i] = t * f32x2{__builtin_amdgcn_rcpf(d[0]), __builtin_amdgcn_rcpf(d[1])};
  }
  u32x2 o = {pk2(v[0][0], v[0][1]), pk2(v[1][0], v[1][1])};
  asm volatile("" : "+v"(o));
  SCHED();
  return o;
}
DI void gelu_block(const f32x16& acc, float k1, float k0, float ia, const Ld& L, unsigned voff, unsigned soff) {
  const u32x2 a = gelu4(acc, 0, k1, k0, ia), b = gelu4(acc, 1, k1, k0, ia);
  stsc(__builtin_shufflevector(a, b, 0, 1, 2, 3), L.rws, voff, soff);
  const u32x2 c = gelu4(acc, 2, k1, k0, ia), d = gelu4(acc, 3, k1, k0, ia);
  stsc(__builtin_shufflevector(c, d, 0, 1, 2, 3), L.rws, voff, soff + 1024);
}

template <int NB, class PRO>
DI void unit_w1(Ld& L, PRO rest, unsigned o0, u64& tag, unsigned xa0, unsigned xa1, unsigned wl, unsigned par, unsigned lane,
                unsigned hsoff, bf16x8 (&w2r)[9][2], unsigned a20, unsigned a21, unsigned vfo) {
  constexpr unsigned b0 = NB == 4 ? 0 : 4;
  f32x16 acc[NB]; unsigned wa[NB];
#pragma unroll
  for (int b = 0; b < NB; ++b) wa[b] = wl + ((o0 + (b0 + b) * 64) ^ par);
  float r, a, ia;
  kloop<NB, true, true, true, 3584, 32, 64, 96>(acc, wa, xa0, xa1, L, rest, tag, r, a, ia);
  const float k0 = -2.302208198144325f * a, k1 = -0.10294323970248637f * a * a * a;
  hsoff += b0 * 2048;
  issue_w2<2>(L);
  PRIO(3);
  gelu_block(acc[0], k1, k0, ia, L, lane * 16, hsoff);
  gelu_block(acc[1], k1, k0, ia, L, lane * 16, hsoff + 2048);
  PRIO(2);
  WAITV(10);
  w2_to_regs<0, 6>(w2r, a20, a21);
  gelu_block(acc[2], k1, k0, ia, L, lane * 16, hsoff + 4096);
  WAITV(2);
  wave_flag(L.rws, vfo, 0, tag);
  PRIO(0);
  w2_to_regs<6, 9>(w2r, a20, a21);
  if (NB == 4) gelu_block(acc[NB - 1], k1, k0, ia, L, lane * 16, hsoff + 6144);
  LGKM0();
  BAR();
  WAITV(0);
  wave_flag(L.rws, vfo, 1, tag);
}
template <class PRO>
DI void unit_q(Ld& L, PRO rest, unsigned o0, unsigned b0, u64& tag, unsigned xa0, unsigned xa1, unsigned wl, unsigned par, unsigned lane, unsigned qsoff) {
  f32x16 acc[4]; unsigned wa[4];
#pragma unroll
  for (int b = 0; b < 4; ++b) wa[b] = wl + ((o0 + (b0 + b) * 56) ^ par);
  float r, a, ia;
  kloop<4, true, false, false, 2688, 28, 56, 84>(acc, wa, xa0, xa1, L, rest, tag, r, a, ia);
  const float rl = r * 1.4426950408889634f;
  unsigned lm = lane < 32 ? ~0u : 0u;
  asm volatile("" : "+v"(lm));
#pragma unroll
  for (int b = 0; b < 4; ++b) {
    float v[16];
#pragma unroll
    for (int i = 0; i < 16; ++i) v[i] = phi(acc[b][i], r, rl);
    u32x4 p0 = {pk2(v[0], v[1]), pk2(v[4], v[5]), pk2(v[6], v[7]), pk2(v[2], v[3])};
    u32x4 p1 = {pk2(v[8], v[9]), pk2(v[12], v[13]) & lm, pk2(v[14], v[15]) & lm, pk2(v[10], v[11])};
    stsc0(p0, L.rws, lane * 16, qsoff + (b0 + b) * 2048);
    stsc(p1, L.rws, lane * 16, qsoff + (b0 + b) * 2048 + 1024);
    SCHED();
  }
}
template <int NH, class PRO>
DI void unit_kv(Ld& L, PRO rest, unsigned o0, unsigned h0, u64& tag, unsigned xa0, unsigned xa1, unsigned wl, unsigned par, unsigned lane,
                bf16x8 (&w2r)[9][2], unsigned a20, unsigned a21) {
  f32x16 acc[2 * NH]; unsigned wa[2 * NH];
#pragma unroll
  for (int b = 0; b < 2 * NH; ++b) wa[b] = wl + ((o0 + (h0 + (b >> 1)) * 56 + (b & 1) * 256) ^ par);
  float r, a, ia;
  kloop<2 * NH, false, false, true, 2688, 448, 28, 476>(acc, wa, xa0, xa1, L, rest, tag, r, a, ia);
  const unsigned l32 = lane & 31, hh = lane >> 5, g = L.w & 3;
  *(LAS float*)(L_RS + (g * 32 + l32) * 4) = r;
  WAITV(6);
  w2_to_regs<0, 3>(w2r, a20, a21);
  LGKM0();
  f32x4 ra[4];
#pragma unroll
  for (int q = 0; q < 4; ++q) ra[q] = ldf4(L_RS + (g * 32 + q * 8 + hh * 4) * 4);
  SCHED();
  issue_w2<2, 0>(L);
  SCHED();
  const unsigned vpad = l32 == 28 ? 0x3f803f80u : 0u;
#pragma unroll
  for (int h = 0; h < NH; ++h) {
    f32x16 kv;
#pragma unroll
    for (int i = 0; i < 16; ++i) kv[i] = 0.f;
#pragma unroll
    for (int s = 0; s < 2; ++s) {
      u32x4 pk, pv;
#pragma unroll
      for (int e = 0; e < 4; ++e) {
        const int i = 8 * s + 2 * e;
        const float r0 = ra[i >> 2][i & 3], r1 = ra[i >> 2][(i & 3) + 1];
        const unsigned kk = pk2(phi(acc[2 * h][i], r0, r0 * 1.4426950408889634f), phi(acc[2 * h][i + 1], r1, r1 * 1.4426950408889634f));
        const unsigned vv = pk2(r0 * acc[2 * h + 1][i], r1 * acc[2 * h + 1][i + 1]);
        pk[e] = l32 < 28 ? kk : 0u;
        pv[e] = l32 < 28 ? vv : vpad;
      }
      kv = mfmau(pk, pv, kv);
    }
#pragma unroll
    for (int q = 0; q < 4; ++q) {
      f32x4 t = {kv[4 * q], kv[4 * q + 1], kv[4 * q + 2], kv[4 * q + 3]};
      *(LAS f32x4*)(2 * STG + (((h0 + h) * 4 + g) * 4 + q) * 1024 + lane * 16) = t;
    }
  }
  WAITL0();
}
constexpr u64 lut8(u64 a, u64 b, u64 c, u64 d, u64 e, u64 f, u64 g, u64 h) { return a | b << 8 | c << 16 | d << 24 | e << 32 | f << 40 | g << 48 | h << 56; }
constexpr u64 lut5(u64 a, u64 b, u64 c, u64 d, u64 e, u64 f, u64 g, u64 h, u64 i, u64 j, u64 k) {
  return a | b << 5 | c << 10 | d << 15 | e << 20 | f << 25 | g << 30 | h << 35 | i << 40 | j << 45 | k << 50;
}

__global__ __launch_bounds__(512, 2) void fused_kernel(const char* x_, const char* wqkv_, const char* wo_, const char* w1_, const char* b1_, const char* w2_,
                                                       const char* b2_, char* out_, char* ws_) {
  extern __shared__ __attribute__((aligned(16))) char lds_dyn[];
  u64x4 ka, kb; u64 kc;
  asm volatile("s_load_dwordx8 %0, %3, 0x0\n\ts_load_dwordx8 %1, %3, 0x20\n\ts_load_dwordx2 %2, %3, 0x40"
               : "=&s"(ka), "=&s"(kb), "=&s"(kc) : "s"(__builtin_amdgcn_kernarg_segment_ptr()));
  SCHED();
  const unsigned tid = threadIdx.x, lane = tid & 63, w = __builtin_amdgcn_readfirstlane(tid >> 6);
  const unsigned xcd = blockIdx.x & 7, bi = blockIdx.x >> 3;
  {
    u64 pc;
    asm volatile("s_getpc_b64 %0" : "=s"(pc));
    touch<0>(((bi * 8 + w) * 2 + (lane & 1)) * 128, __builtin_amdgcn_make_buffer_rsrc((void*)(pc & ~127ull), 0, CODE_BYTES, 0x00020000));
    SCHED();
  }
  const unsigned pg = xcd >> 1, half = xcd & 1, panel = pg * 4 + (bi >> 3), jj = bi & 7;
  const bool is_w1 = jj < 4, is_q = jj == 7;
  const unsigned seg0 = (unsigned)((half ? lut8(14, 17, 21, 24, 10, 11, 12, 3) : lut8(0, 3, 7, 10, 7, 8, 9, 0)) >> (8 * jj) & 255) * 128;
  const unsigned seg1 = seg0 + ((jj - 4 < 3) ? 896 : 256), rs = is_w1 ? 3584 : 2688;
  Ld L;
  L.w = w;
  L.lw = w * 4352; L.lx = w * 2080; L.l2 = w * 6144;
  const unsigned r8 = lane >> 3, c16 = ((lane & 7) ^ ((lane >> 2) & 4)) * 16;
  {
    L.vx = (w * 16 + r8) * 896 + c16;
    unsigned rp = lane >> 5, sp = ((lane & 31) * 16) ^ (rp * 128);
    L.vw = (w * 8 + rp) * rs + ((sp & 256) ? seg1 : seg0) + (sp & 255);
  }
  asm volatile("" : "+v"(L.vx), "+v"(L.vw), "+s"(L.lw), "+s"(L.lx));
  SCHED();
  asm volatile("s_waitcnt lgkmcnt(0)" : "+s"(ka), "+s"(kb), "+s"(kc) : "s"(__builtin_amdgcn_kernarg_segment_ptr()));
  const char *x = (const char*)ka[0], *wqkv = (const char*)ka[1], *wo = (const char*)ka[2], *w1 = (const char*)ka[3];
  const char *b1 = (const char*)kb[0], *w2 = (const char*)kb[1], *b2 = (const char*)kb[2];
  char *out = (char*)kb[3], *ws = (char*)kc;
  L.ws = ws;
  L.rx = __builtin_amdgcn_make_buffer_rsrc((void*)(x + panel * 128 * 896), 0, 128 * 896, 0x00020000);
  L.rw = __builtin_amdgcn_make_buffer_rsrc((void*)(is_w1 ? w1 : wqkv), 0, 448 * rs, 0x00020000);
  SCHED();
  if (is_w1) issue_chunk<3584, 0>(L);
  else issue_chunk<2688, 0>(L);
  SCHED();
  const unsigned slot = (unsigned)((half ? lut8(4, 5, 6, 7, 11, 12, 13, 15) : lut8(0, 1, 2, 3, 8, 9, 10, 14)) >> (8 * jj) & 255);
  const unsigned c28 = (unsigned)((half ? lut8(32, 40, 48, 56, 88, 90, 93, 72) : lut8(0, 8, 16, 24, 80, 83, 86, 64)) >> (8 * jj) & 255);
  unsigned o0 = (unsigned)((half ? lut8(0, 8, 0, 8, 8, 6, 11, 8) : lut8(0, 8, 0, 8, 0, 5, 10, 0)) >> (8 * jj) & 255) * 8;
  const unsigned T = xcd * 4 + (bi >> 3), ct = jj;
  const unsigned kvh0 = c28 - 80;
  const bool three = ((jj - half) >> 1) == 2;
  const unsigned grp = w >> 2, h0 = grp * (three ? 2 : 1);
  const unsigned c0 = c28 * 28 + grp * (is_w1 ? 128 : (is_q ? 112 : (three ? 56 : 28)));
  const unsigned tk = (bi >> 3) * 4 + (jj & 3), li = (pg >> 1) * 13 + tk;
  const bool tw1 = li < 14, ton = tk < 13 && li < 25 - half;
  const unsigned tq = (unsigned)((half ? lut5(4, 5, 6, 11, 12, 13, 17, 18, 19, 20, 0) : lut5(0, 1, 2, 3, 7, 8, 9, 10, 14, 15, 16)) >> (5 * (li - 14) & 63) & 31);
  const unsigned tl = tw1 ? half * 14 + li : tq, lc = ton ? (tw1 ? tl : 28 + tq) : 49, trs = tw1 ? 3584 : 2688;
  const rsrc_t rt = __builtin_amdgcn_make_buffer_rsrc((void*)(tw1 ? w1 : wqkv), 0, (ton && w < 7) ? 448 * trs : 0, 0x00020000);
  const unsigned srow = (slot & 7) * 16 + w * 2 + (lane >> 5);
  unsigned vxr = (lane & 31) < 28 ? srow * 896 + (lane & 31) * 32 : OOB;
  unsigned vc = ((pg & 1) * 224 + w * 32 + r8) * trs + tl * 128 + (lane & 7) * 16;
  unsigned zero = 0, bo = (c0 + (lane & 31)) * 2;
  unsigned bo3 = grp ? bo : bo + 192;
  auto rest1 = [&]() {
    asm volatile("global_load_dwordx2 %0, %5, %6 sc1\n\tglobal_load_ushort %1, %7, %8\n\tglobal_load_ushort %2, %7, %8 offset:64\n\t"
                 "global_load_ushort %3, %7, %8 offset:128\n\tglobal_load_ushort %4, %9, %8"
                 : "=&v"(L.gen), "=&v"(L.bb[0]), "=&v"(L.bb[1]), "=&v"(L.bb[2]), "=&v"(L.bb[3]) : "v"(zero), "s"(ws), "v"(bo), "s"(b1), "v"(bo3) : "memory");
  };
  auto rest = [&]() {
    asm volatile("s_nop 4\n\tglobal_load_dwordx2 %0, %1, %2 sc1" : "=&v"(L.gen) : "v"(zero), "s"(ws) : "memory");
    asm volatile("buffer_load_dwordx4 %0, %4, %5, 0 offen\n\tbuffer_load_dwordx4 %1, %4, %5, %6 offen\n\t"
                 "buffer_load_dwordx4 %2, %4, %5, %7 offen\n\tbuffer_load_dwordx4 %3, %4, %5, %8 offen"
                 : "=&v"(L.cr[0]), "=&v"(L.cr[1]), "=&v"(L.cr[2]), "=&v"(L.cr[3]) : "v"(vc), "s"(rt), "s"(8 * trs), "s"(16 * trs), "s"(24 * trs) : "memory");
    asm volatile("buffer_load_dwordx4 %0, %2, %3, 0 offen\n\tbuffer_load_dwordx4 %1, %2, %3, 0 offen offset:16"
                 : "=&v"(L.xr[0]), "=&v"(L.xr[1]) : "v"(vxr), "s"(L.rx) : "memory");
  };
  L.r2 = __builtin_amdgcn_make_buffer_rsrc((void*)w2, 0, 1792 * 896, 0x00020000);
  L.ro = __builtin_amdgcn_make_buffer_rsrc((void*)wo, 0, 448 * 896, 0x00020000);
  L.rws = __builtin_amdgcn_make_buffer_rsrc((void*)ws, 0, W_END, 0x00020000);
  const unsigned u = (w + (ct + 2 * T)) & 7;
  L.v2 = (u * 224 + r8) * 896 + c16 + ct * 128;
  L.vo = (u * 56 + r8) * 896 + c16 + ct * 128;
  L.vob = (u * 56 + (r8 & 3)) * 896 + c16 + ct * 128;
  L.vg2 = ((blockIdx.x * 8 + w) * 7 + min(lane, 6u)) * 128;
  L.vgo = ((blockIdx.x * 8 + w) * 2 + (lane & 1)) * 128;
  L.rout = __builtin_amdgcn_make_buffer_rsrc((void*)out, 0, 2048 * 896, 0x00020000);
  const unsigned l32 = lane & 31, hh = lane >> 5, q4 = (lane & 15) >> 2, blk = (lane >> 4) & 1, p4 = lane & 3;
  L.vrs = W_RS + (panel * 128 + (w & 3) * 32 + l32) * 16;
  L.vcs = W_CS + (c0 + l32) * 16 + hh * 8;
  L.acq = L_CQ + (c0 - c28 * 28 + l32) * 16 + hh * 8;
  L.lsl = L_CQ + (grp ? 4 + (w & 1) : w) * 1024;
  L.vsl = (grp ? W_RS + (panel * 128 + (w & 1) * 64) * 16 : W_CS + (c28 * 28 + (w & 1) * 64 + (w >> 1) * ((jj - 4 < 3) ? 448 : 128)) * 16) + lane * 16;
  L.vsr = l32 < 2 ? W_RS + (panel * 128 + srow) * 16 + l32 * 8 : OOB;
  L.vsc = lane < 8 ? W_CS + (lc * 64 + w * 8 + lane) * 16 + (pg & 1) * 8 : OOB;
  unsigned xa0, xa1;
  {
    unsigned r = (w & 3) * 32 + l32, f = (r >> 1) & 1, base = 2 * STG + XOFF + (r >> 3) * 1040 + (r & 7) * 128 + hh * 16;
    xa0 = base + f * 64; xa1 = base + (f ^ 1) * 64;
  }
  const unsigned par = (q4 & 1) * 128, wl = 2 * STG + 1088 * (4 * hh + (q4 >> 1)) + (q4 & 1) * 512;
  o0 += blk * 32 + p4 * 8;
  const unsigned a20 = (4 * hh + q4) * 128 + (q4 >> 1) * 64 + blk * 32 + p4 * 8 + w * 6144, a21 = a20 ^ 64;

  bf16x8 w2r[9][2];
  u64 tag = 0;
  const unsigned p2 = T >> 1, tg0 = (T & 1) * 2 + (lane & 1);
  const unsigned vfo = lane ? OOB : W_FL + ((panel * 16 + slot) * 8 + w) * 128;
  const unsigned vfa = W_FL + ((p2 * 16 + u) * 8 + tg0 + (lane & 2) * 2) * 128;
  if (is_w1) {
    asm volatile("" : "+v"(L.v2), "+v"(L.vo), "+v"(L.vob), "+v"(L.vsl), "+v"(L.acq), "+v"(bo), "+v"(bo3));
    unsigned hsoff = W_H + ((panel * 4 + (w & 3)) * 112 + slot * 14) * 1024;
    if (grp == 0) unit_w1<4>(L, rest1, o0, tag, xa0, xa1, wl, par, lane, hsoff, w2r, a20, a21, vfo);
    else unit_w1<3>(L, rest1, o0, tag, xa0, xa1, wl, par, lane, hsoff, w2r, a20, a21, vfo);
  } else if (is_q) {
    unsigned qsoff = W_PQ + ((panel * 4 + (w & 3)) * 16 + half * 8) * 2048;
    unit_q(L, rest, o0, grp * 4, tag, xa0, xa1, wl, par, lane, qsoff);
    WAITV(0);
    wave_flag(L.rws, vfo, 1, tag);
    const unsigned rj = panel * 2 + half, tg = (unsigned)tag;
    if (w < 2) {
      u32x4 t[16];
#pragma unroll 1
      for (int sp = 0; sp < SPIN_MAX; ++sp) {
        unsigned m = 0;
#pragma unroll
        for (int p = 0; p < 16; ++p) t[p] = ldsc(L.rws, lane * 16, W_KVP + p * 65536 + (rj * 2 + w) * 1024);
#pragma unroll
        for (int p = 0; p < 16; ++p) m |= neq2(t[p][1], t[p][2], tg);
        asm volatile("" : "+v"(m)::"memory");
        if (__all(m == 0)) break;
      }
      f32x4 s0 = {flo(t[0][0]), fhi(t[0][0]), flo(t[0][3]), fhi(t[0][3])};
#pragma unroll
      for (int p = 1; p < 16; ++p) s0 += f32x4{flo(t[p][0]), fhi(t[p][0]), flo(t[p][3]), fhi(t[p][3])};
      stsc(u32x4{pk2(s0[0], s0[1]), tg, tg, pk2(s0[2], s0[3])}, L.rws, lane * 16, W_KVF + (rj * 2 + w) * 1024);
    } else if (w == 2 && rj == 0) {
#pragma unroll 1
      for (int k = 0; k < 4; ++k) wait_flag(ws, W_FL + (k * 64 + lane) * 1024 + 8, tag);
      if (lane == 0) set_flag(ws, W_GEN, tag);
    }
    return;
  } else {
    if (grp == 0 && three) unit_kv<2>(L, rest, o0, 0, tag, xa0, xa1, wl, par, lane, w2r, a20, a21);
    else unit_kv<1>(L, rest, o0, h0, tag, xa0, xa1, wl, par, lane, w2r, a20, a21);
    BAR();
#pragma unroll
    for (unsigned k = 0; k < 2; ++k) {
      const unsigned it = k ? 8 + (w & 3) : w, a = 2 * STG + ((it >> 2) * 16 + (it & 3)) * 1024 + lane * 16;
      f32x4 s = (ldf4(a) + ldf4(a + 4096)) + (ldf4(a + 8192) + ldf4(a + 12288));
      stsc(u32x4{pk2(s[0], s[1]), (unsigned)tag, (unsigned)tag, pk2(s[2], s[3])}, L.rws, lane * 16,
           (k && !(three && grp == 0)) ? OOB : W_KVP + (panel * 16 + kvh0) * 4096 + it * 1024);
    }
    SCHED();
    WAITV(2);
    w2_to_regs<3, 9, 2>(w2r, a20, a21);
    wave_flag(L.rws, vfo, 1, tag);
    LGKM0();
    BAR();
  }
  const unsigned vfq = (lane & 4) ? W_FL + ((p2 * 16 + 14 + (u >> 2)) * 8 + tg0 + (u & 2) * 2) * 128 + 8 : vfa + 8;
  u32x2 pf;
  issue_w2<3>(L);
  asm volatile("buffer_load_dwordx2 %0, %1, %2, 0 offen sc1" : "=&v"(pf) : "v"((lane & 12) ? vfq : vfa), "s"(L.rws) : "memory");
  issue_w2<4>(L);
  asm volatile("s_waitcnt vmcnt(6)" : "+v"(pf)::"memory");
  const unsigned okm = (unsigned)__builtin_amdgcn_ballot_w64(pf[0] == (unsigned)tag && pf[1] == (unsigned)(tag >> 32));
  if ((okm & 15) != 15) wait_flag(ws, vfa, tag);

  f32x16 acc[2][2];
#pragma unroll
  for (int i = 0; i < 4; ++i)
#pragma unroll
    for (int r = 0; r < 16; ++r) acc[i >> 1][i & 1][r] = 0.f;
  const unsigned hb = W_H + (T * 2 * 112 + u * 14) * 1024;
  auto hstep = [&](int n, const u32x4& h0_, const u32x4& h1_) {
    const bf16x8 wa = w2r[n][0], wb = w2r[n][1], ha = __builtin_bit_cast(bf16x8, h0_), hb_ = __builtin_bit_cast(bf16x8, h1_);
    acc[0][0] = mfma(wa, ha, acc[0][0]);
    acc[0][1] = mfma(wb, ha, acc[0][1]);
    acc[1][0] = mfma(wa, hb_, acc[1][0]);
    acc[1][1] = mfma(wb, hb_, acc[1][1]);
    asm volatile("" : "+v"(acc[0][0]), "+v"(acc[0][1]), "+v"(acc[1][0]), "+v"(acc[1][1]));
    SCHED();
  };
  u32x4 ha[8][2], hc[6][2], pq[2][2][2], kf[2][2], kg[8];
#pragma unroll
  for (int n = 0; n < 8; ++n) { const int k = slot_step(n); ha[n][0] = ldsc(L.rws, lane * 16, hb + k * 1024); ha[n][1] = ldsc(L.rws, lane * 16, hb + (112 + k) * 1024); }
  SCHED();
  if (okm != ~0u) asm volatile("buffer_load_dwordx2 %0, %1, %2, 0 offen sc1" : "+v"(pf) : "v"(vfq), "s"(L.rws) : "memory");
  SCHED();
  hstep(0, ha[0][0], ha[0][1]); hstep(1, ha[1][0], ha[1][1]); hstep(2, ha[2][0], ha[2][1]); hstep(3, ha[3][0], ha[3][1]);
  if (okm != ~0u) {
    asm volatile("s_waitcnt vmcnt(0)" : "+v"(pf)::"memory");
    if (!__all(pf[0] == (unsigned)tag && pf[1] == (unsigned)(tag >> 32))) wait_flag(ws, vfq, tag);
  }
#pragma unroll
  for (int i = 0; i < 8; ++i) kg[i] = ldsc(L.rws, lane * 16, W_KVF + (u * 8 + i) * 1024);
#pragma unroll
  for (int i = 0; i < 8; ++i) pq[(i >> 1) & 1][i >> 2][i & 1] = ldsc(L.rws, lane * 16, W_PQ + (((T * 2 + (i >> 2)) * 16 + u * 2) * 2 + (i & 3)) * 1024);
  SCHED();
  hstep(4, ha[4][0], ha[4][1]); hstep(5, ha[5][0], ha[5][1]); hstep(6, ha[6][0], ha[6][1]); hstep(7, ha[7][0], ha[7][1]);
  issue_w2<5>(L);
  SCHED();
#pragma unroll
  for (int th = 0; th < 2 - IL; ++th)
#pragma unroll
    for (int n = 0; n < 6; ++n) hc[n][th] = ldsc(L.rws, lane * 16, hb + (th * 112 + slot_step(n ? n + 12 : 8)) * 1024);
  bf16x8 wf[2][2][2];
#pragma unroll
  for (int n = 0; n < 4; ++n) {
    wf[n >> 1][n & 1][0] = ldtr(a20 + (n / 3) * STG + (n % 3) * 2048, 1024);
    wf[n >> 1][n & 1][1] = ldtr(a21 + (n / 3) * STG + (n % 3) * 2048, 1024);
  }
  SCHED();
#pragma unroll 1
  for (int sp = 0; sp < SPIN_MAX; ++sp) {
    unsigned m = 0;
#pragma unroll
    for (int i = 0; i < 8; ++i) m |= neq2(kg[i][1], kg[i][2], (unsigned)tag);
    asm volatile("" : "+v"(m));
    if (__all(m == 0)) break;
    const unsigned vk = W_KVF + u * 8192 + lane * 16;
#pragma unroll
    for (int i = 0; i < 8; i += 4)
      asm volatile("buffer_load_dwordx4 %0, %4, %5, %6 offen sc1\n\tbuffer_load_dwordx4 %1, %4, %5, %6 offen offset:1024 sc1\n\t"
                   "buffer_load_dwordx4 %2, %4, %5, %6 offen offset:2048 sc1\n\tbuffer_load_dwordx4 %3, %4, %5, %6 offen offset:3072 sc1"
                   : "=&v"(kg[i]), "=&v"(kg[i + 1]), "=&v"(kg[i + 2]), "=&v"(kg[i + 3]) : "v"(vk), "s"(L.rws), "s"(i * 1024) : "memory");
    WAITV(0);
    SCHED();
    asm volatile("" : "+v"(kg[0]), "+v"(kg[1]), "+v"(kg[2]), "+v"(kg[3]), "+v"(kg[4]), "+v"(kg[5]), "+v"(kg[6]), "+v"(kg[7]));
  }
#pragma unroll
  for (int i = 0; i < 4; ++i) kf[i >> 1][i & 1] = u32x4{kg[2 * i][0], kg[2 * i + 1][0], kg[2 * i + 1][3], kg[2 * i][3]};
  auto numer = [&](int th, int hd) {
    f32x16 nm;
#pragma unroll
    for (int r = 0; r < 16; ++r) nm[r] = 0.f;
    nm = mfmau(kf[hd][0], pq[hd][th][0], nm);
    nm = mfmau(kf[hd][1], pq[hd][th][1], nm);
    asm volatile("" : "+v"(nm));
    return nm;
  };
  auto scale = [&](const f32x16& nm, u32x4& po0, u32x4& po1) {
    const float rd = __builtin_amdgcn_rcpf(bcast_hi(nm[12]) + 1e-6f);
    po0 = u32x4{pk2(nm[0] * rd, nm[1] * rd), pk2(nm[2] * rd, nm[3] * rd), pk2(nm[4] * rd, nm[5] * rd), pk2(nm[6] * rd, nm[7] * rd)};
    po1 = u32x4{pk2(nm[8] * rd, nm[9] * rd), pk2(nm[10] * rd, nm[11] * rd), hh ? 0u : pk2(nm[12] * rd, nm[13] * rd), hh ? 0u : pk2(nm[14] * rd, nm[15] * rd)};
    asm volatile("" : "+v"(po0), "+v"(po1));
    SCHED();
  };
  auto proj = [&](int th, int hd, const u32x4& po0, const u32x4& po1) {
    acc[th][0] = mfma(wf[hd][0][0], __builtin_bit_cast(bf16x8, po0), acc[th][0]);
    acc[th][1] = mfma(wf[hd][0][1], __builtin_bit_cast(bf16x8, po0), acc[th][1]);
    acc[th][0] = mfma(wf[hd][1][0], __builtin_bit_cast(bf16x8, po1), acc[th][0]);
    acc[th][1] = mfma(wf[hd][1][1], __builtin_bit_cast(bf16x8, po1), acc[th][1]);
    asm volatile("" : "+v"(acc[th][0]), "+v"(acc[th][1]));
    SCHED();
  };
  auto park = [&](int th) {
    const unsigned ra = th * STG + w * 6144 + l32 * 144 + hh * 16;
#pragma unroll
    for (int jp = 0; jp < 4; ++jp) {
      const f32x16& c = acc[th][jp >> 1];
      const int o = (jp & 1) * 8;
      *(LAS u32x4*)(ra + jp * 32) = u32x4{pk2(c[o], c[o + 1]), pk2(c[o + 2], c[o + 3]), pk2(c[o + 4], c[o + 5]), pk2(c[o + 6], c[o + 7])};
    }
    SCHED();
  };
  const unsigned otok = T * 64 + (tid >> 3), ocol = ct * 64 + (tid & 7) * 8;
  u32x4 xin, bin;
  {
    u32x4 pa0, pa1, pb0, pb1;
    f32x16 n00 = numer(0, 0), n01 = numer(0, 1);
    SCHED();
    scale(n00, pa0, pa1);
    f32x16 n10 = numer(1, 0);
    proj(0, 0, pa0, pa1);
    if (IL) {
#pragma unroll
      for (int n = 0; n < 6; ++n) hc[n][1] = ldsc(L.rws, lane * 16, hb + (112 + slot_step(n ? n + 12 : 8)) * 1024);
      SCHED();
    }
    scale(n01, pb0, pb1);
    f32x16 n11 = numer(1, 1);
    proj(0, 1, pb0, pb1);
    WAITV(12);
    xin = *(const GAS u32x4*)(x + (otok * 448 + ocol) * 2);
    bin = *(const GAS u32x4*)(b2 + ocol * 2);
    bf16x8 wb[6][2];
    wb[0][0] = w2r[8][0]; wb[0][1] = w2r[8][1];
#pragma unroll
    for (int n = 4; n < 9; ++n) {
      wb[n - 3][0] = ldtr(a20 + (n / 3) * STG + (n % 3) * 2048, 1024);
      wb[n - 3][1] = ldtr(a21 + (n / 3) * STG + (n % 3) * 2048, 1024);
    }
    scale(n10, pa0, pa1);
    proj(1, 0, pa0, pa1);
    scale(n11, pb0, pb1);
    LGKM0();
#pragma unroll
    for (int th = 0; th < 2; ++th) {
#pragma unroll
      for (int n = 0; n < 6; ++n) {
        acc[th][0] = mfma(wb[n][0], __builtin_bit_cast(bf16x8, hc[n][th]), acc[th][0]);
        acc[th][1] = mfma(wb[n][1], __builtin_bit_cast(bf16x8, hc[n][th]), acc[th][1]);
        asm volatile("" : "+v"(acc[th][0]), "+v"(acc[th][1]));
        SCHED();
      }
      if (th == 0) { proj(1, 1, pb0, pb1); park(0); }
    }
    park(1);
  }
  float xb[8];
#pragma unroll
  for (int i = 0; i < 4; ++i) { xb[2 * i] = flo(xin[i]) + flo(bin[i]); xb[2 * i + 1] = fhi(xin[i]) + fhi(bin[i]); }
  asm volatile("" : "+v"(xb[0]), "+v"(xb[1]), "+v"(xb[2]), "+v"(xb[3]), "+v"(xb[4]), "+v"(xb[5]), "+v"(xb[6]), "+v"(xb[7]));
  WAITL0();
  BAR();
  {
    const unsigned row = tid >> 3, a = (row >> 5) * STG + (row & 31) * 144 + (tid & 7) * 16;
    u32x4 pp[8];
#pragma unroll
    for (int p = 0; p < 8; ++p) pp[p] = *(LAS u32x4*)(a + p * 6144);
    unsigned c0 = 0x00003f80u, c1 = 0x3f800000u;
    asm volatile("" : "+s"(c0), "+s"(c1));
    const bf16x2 e0 = __builtin_bit_cast(bf16x2, c0), e1 = __builtin_bit_cast(bf16x2, c1);
    float s[8];
#pragma unroll
    for (int i = 0; i < 4; ++i) { s[2 * i] = flo(pp[0][i]); s[2 * i + 1] = fhi(pp[0][i]); }
#pragma unroll
    for (int p = 1; p < 8; ++p)
#pragma unroll
      for (int i = 0; i < 4; ++i) {
        const unsigned d = pp[p][i];
        s[2 * i] = __builtin_amdgcn_fdot2_f32_bf16(__builtin_bit_cast(bf16x2, d), e0, s[2 * i], false);
        s[2 * i + 1] = __builtin_amdgcn_fdot2_f32_bf16(__builtin_bit_cast(bf16x2, d), e1, s[2 * i + 1], false);
      }
    f32x4 s0, s1;
    const bool odd = tid & 1;
#pragma unroll
    for (int i = 0; i < 4; ++i) {
      const float g = __int_as_float(__builtin_amdgcn_update_dpp(0, __float_as_int(odd ? s[i] : s[4 + i]), 0xB1, 0xF, 0xF, true));
      s0[i] = odd ? g : s[i];
      s1[i] = odd ? s[4 + i] : g;
    }
    float r[8];
#pragma unroll
    for (int i = 0; i < 4; ++i) { r[i] = xb[i] + s0[i]; r[4 + i] = xb[4 + i] + s1[i]; }
    u32x4 o = {pk2(r[0], r[1]), pk2(r[2], r[3]), pk2(r[4], r[5]), pk2(r[6], r[7])};
    __builtin_amdgcn_raw_buffer_store_b128(o, L.rout, (otok * 448 + ocol) * 2, 0, 16);
  }
}

extern "C" void kernel_launch(void* const* d_in, const int* in_sizes, int n_in, void* d_out, int out_size, void* d_ws, size_t ws_size,
                              hipStream_t stream) {
  (void)hipFuncSetAttribute((const void*)fused_kernel, hipFuncAttributeMaxDynamicSharedMemorySize, LDS_BYTES);
  fused_kernel<<<256, 512, LDS_BYTES, stream>>>((const char*)d_in[0], (const char*)d_in[1], (const char*)d_in[2], (const char*)d_in[4],
                                                (const char*)d_in[5], (const char*)d_in[6], (const char*)d_in[7], (char*)d_out, (char*)d_ws);
}
```
